```python
import torch

def _softmax(x, axis=-1):
    return torch.softmax(x, dim=axis)
def _gelu(x):
    return torch.nn.functional.gelu(x, approximate="tanh")
def _silu(x):
    return torch.nn.functional.silu(x)
def _sigmoid(x):
    return torch.sigmoid(x)
def _layernorm(x, axis=-1, eps=1e-5):
    m = x.mean(dim=axis, keepdim=True)
    v = x.var(dim=axis, keepdim=True, unbiased=False)
    return (x - m) / torch.sqrt(v + eps)
def _scatter_add(tgt, idx, src, axis=0):
    return tgt.clone().index_add_(axis, idx, src)
def _segment_sum(src, seg, n):
    out = torch.zeros((n,) + src.shape[1:], dtype=src.dtype)
    return out.index_add_(0, seg, src)
def _pad(x, pw):
    flat = tuple(v for pair in reversed(pw) for v in pair)
    return torch.nn.functional.pad(x, flat)

kernel_name = 'recurrent_scan__ssm_conv_h3__mixer_channel_mlp__prompt'

def setup_inputs():

    _w = (lambda a: (lambda t: t.to(torch.bfloat16) if t.is_floating_point() else t)(torch.as_tensor(a)))
    def _A_setup():
        B, S, D = 1, 2048, 512
        ks = 4
        return {
            "q": _w((lambda *s: torch.randn(*s, dtype=torch.bfloat16) * 0.05)(B, S, D)),
            "k": _w((lambda *s: torch.randn(*s, dtype=torch.bfloat16) * 0.8)(B, S, D)),
            "v": _w((lambda *s: torch.randn(*s, dtype=torch.bfloat16) * 0.8)(B, S, D)),
            "shift_k": _w((lambda *s: torch.randn(*s, dtype=torch.bfloat16) * 0.8)(D, ks) * 0.5),
            "ssm_k": _w((lambda *s: torch.randn(*s, dtype=torch.bfloat16) * 0.8)(D, S) * (1.0 / S ** 0.5)),
            "ssm_D": _w((lambda *s: torch.randn(*s, dtype=torch.bfloat16) * 0.8)(D)),
        }
    _A_in = _A_setup()

    def _B_setup():
        B, S, D, F = 1, 2048, 512, 1376
        return {
            "x": _w((lambda *s: torch.randn(*s, dtype=torch.bfloat16) * 0.05)(B, S, D)),
            "w1": _w((lambda *s: torch.randn(*s, dtype=torch.bfloat16) * 0.8)(D, F) * (D ** -0.5)),
            "b1": _w((lambda *s: torch.randn(*s, dtype=torch.bfloat16) * 0.4)(F)),
            "w2": _w((lambda *s: torch.randn(*s, dtype=torch.bfloat16) * 0.2)(F, D) * (F ** -0.5)),
            "b2": _w((lambda *s: torch.randn(*s, dtype=torch.bfloat16) * 0.2)(D)),
        }
    _B_in = _B_setup()

    return {
        "A_q": _A_in["q"],
        "A_k": _A_in["k"],
        "A_v": _A_in["v"],
        "A_shift_k": _A_in["shift_k"],
        "A_ssm_k": _A_in["ssm_k"],
        "A_ssm_D": _A_in["ssm_D"],
        "B_x": _B_in["x"],
        "B_w1": _B_in["w1"],
        "B_b1": _B_in["b1"],
        "B_w2": _B_in["w2"],
        "B_b2": _B_in["b2"],
    }

def reference(A_q, A_k, A_v, A_shift_k, A_ssm_k, A_ssm_D, B_x, B_w1, B_b1, B_w2, B_b2):
    def _A_ref(q, k, v, shift_k, ssm_k, ssm_D):
        B, S, D = q.shape
        ks = shift_k.shape[-1]
        k_pad = _pad(k, ((0, 0), (ks - 1, 0), (0, 0)))
        k_shift = torch.zeros_like(k)
        for t in range(ks):
            k_shift = k_shift + k_pad[:, t:t + S, :] * shift_k[:, ks - 1 - t][None, None, :]
        kv = k_shift * v
        L = 2 * S
        kv_pad = _pad(kv, ((0, 0), (0, S), (0, 0)))
        kf_pad = _pad(ssm_k, ((0, 0), (0, S)))
        kv_f = (lambda x, axes=(-2,-1): torch.fft.rfft2(x.float(), dim=axes))(kv_pad[:, None, :, :], axes=(1, 2))
        kf_f = (lambda x, axes=(-2,-1): torch.fft.rfft2(x.float(), dim=axes))(kf_pad[:, None, :], axes=(1, 2))
        kf_f = (lambda x, *d: x.permute(*d))(kf_f, 1, 2, 0)
        y_f = kv_f * kf_f[None, :, :, :]
        y = (lambda x, s, axes=(-2,-1): torch.fft.irfft2(x, s=s, dim=axes).to(torch.bfloat16))(y_f, (1, L), axes=(1, 2))[:, 0, :S, :]
        y = y + kv * ssm_D[None, None, :]
        return q * y

    def _B_ref(x, w1, b1, w2, b2):
        y = _layernorm(x, axis=-1)
        h = _gelu(torch.einsum("bsd,df->bsf", y, w1) + b1)
        return torch.einsum("bsf,fd->bsd", h, w2) + b2

    x = A_q
    xn = _layernorm(x, axis=-1)
    s = _A_ref(xn, A_k, A_v, A_shift_k, A_ssm_k, A_ssm_D)
    g = _silu(_B_ref(xn, B_w1, B_b1, B_w2, B_b2))
    return x + s * g
```

```cpp
#include <hip/hip_runtime.h>
#include <hip/hip_bf16.h>
typedef __hip_bfloat16 bf16;
constexpr int S=2048, D=512, F=1376;
__device__ __forceinline__ float b2f(bf16 x){return __bfloat162float(x);}
__device__ __forceinline__ float rb(float x){return __bfloat162float(__float2bfloat16(x));}

__global__ void k_ln(const bf16* x, float* xn, float* y2){
  int t=blockIdx.x; int tid=threadIdx.x;
  __shared__ float red[512];
  float v=b2f(x[t*D+tid]);
  red[tid]=v; __syncthreads();
  for(int s=256;s>0;s>>=1){ if(tid<s) red[tid]+=red[tid+s]; __syncthreads(); }
  float m=red[0]/D; __syncthreads();
  red[tid]=(v-m)*(v-m); __syncthreads();
  for(int s=256;s>0;s>>=1){ if(tid<s) red[tid]+=red[tid+s]; __syncthreads(); }
  float var=red[0]/D; __syncthreads();
  float a=(v-m)/sqrtf(var+1e-5f);
  xn[t*D+tid]=a;
  red[tid]=a; __syncthreads();
  for(int s=256;s>0;s>>=1){ if(tid<s) red[tid]+=red[tid+s]; __syncthreads(); }
  float m2=red[0]/D; __syncthreads();
  red[tid]=(a-m2)*(a-m2); __syncthreads();
  for(int s=256;s>0;s>>=1){ if(tid<s) red[tid]+=red[tid+s]; __syncthreads(); }
  float var2=red[0]/D; __syncthreads();
  y2[t*D+tid]=(a-m2)/sqrtf(var2+1e-5f);
}
__global__ void k_kv(const bf16* k,const bf16* v,const bf16* sk,float* kv){
  int t=blockIdx.x,d=threadIdx.x;
  float a=0;
  for(int j=0;j<4;j++){ if(t-j>=0) a+=b2f(k[(t-j)*D+d])*b2f(sk[d*4+j]); }
  kv[t*D+d]=rb(a*b2f(v[t*D+d]));
}
__global__ void k_conv(const float* kv,const bf16* h,const bf16* Dp,float* y){
  int t=blockIdx.x,d=threadIdx.x;
  float a=0;
  for(int j=0;j<=t;j++) a+=b2f(h[d*S+j])*kv[(t-j)*D+d];
  y[t*D+d]=a+kv[t*D+d]*b2f(Dp[d]);
}
__global__ void k_g1(const float* y2,const bf16* w1,const bf16* b1,float* H){
  int t=blockIdx.y; int n=blockIdx.x*64+threadIdx.x; if(n>=F)return;
  float a=0;
  for(int k=0;k<D;k++) a+=y2[t*D+k]*b2f(w1[k*F+n]);
  a+=b2f(b1[n]);
  float g=0.5f*a*(1.f+tanhf(0.7978845608f*(a+0.044715f*a*a*a)));
  H[t*F+n]=rb(g);
}
__global__ void k_g2(const float* H,const bf16* w2,const bf16* b2,const bf16* x,const float* xn,const float* y,bf16* out){
  int t=blockIdx.x,d=threadIdx.x;
  float a=0;
  for(int k=0;k<F;k++) a+=H[t*F+k]*b2f(w2[k*D+d]);
  a+=b2f(b2[d]);
  float g=a/(1.f+expf(-a));
  out[t*D+d]=__float2bfloat16(b2f(x[t*D+d])+xn[t*D+d]*y[t*D+d]*g);
}
extern "C" void kernel_launch(void* const* d_in, const int* in_sizes, int n_in,
                              void* d_out, int out_size, void* d_ws, size_t ws_size,
                              hipStream_t stream) {
  const bf16* q=(const bf16*)d_in[0]; const bf16* k=(const bf16*)d_in[1]; const bf16* v=(const bf16*)d_in[2];
  const bf16* sk=(const bf16*)d_in[3]; const bf16* h=(const bf16*)d_in[4]; const bf16* Dp=(const bf16*)d_in[5];
  const bf16* w1=(const bf16*)d_in[7]; const bf16* b1=(const bf16*)d_in[8]; const bf16* w2=(const bf16*)d_in[9]; const bf16* b2=(const bf16*)d_in[10];
  float* ws=(float*)d_ws;
  float* xn=ws; float* y2=xn+S*D; float* kv=y2+S*D; float* y=kv+S*D; float* H=y+S*D;
  k_ln<<<S,512,0,stream>>>(q,xn,y2);
  k_kv<<<S,512,0,stream>>>(k,v,sk,kv);
  k_conv<<<S,512,0,stream>>>(kv,h,Dp,y);
  k_g1<<<dim3((F+63)/64,S),64,0,stream>>>(y2,w1,b1,H);
  k_g2<<<S,512,0,stream>>>(H,w2,b2,q,xn,y,(bf16*)d_out);
}
```

```cpp
#define EXP_NOST 1
#include <hip/hip_runtime.h>
#include <hip/hip_bf16.h>
#include <stdint.h>

typedef unsigned int u32;
typedef unsigned long long u64;
typedef u32 u32x2 __attribute__((ext_vector_type(2)));
typedef u32 u32x4 __attribute__((ext_vector_type(4)));
typedef u32 u32x8 __attribute__((ext_vector_type(8)));
typedef u32 u32x16 __attribute__((ext_vector_type(16)));
typedef float f32x2 __attribute__((ext_vector_type(2)));
typedef float f32x4 __attribute__((ext_vector_type(4)));
typedef float f32x16 __attribute__((ext_vector_type(16)));
typedef __bf16 bf2 __attribute__((ext_vector_type(2)));
typedef __bf16 bf8 __attribute__((ext_vector_type(8)));
typedef short s16x4 __attribute__((ext_vector_type(4)));
#define AS3 __attribute__((address_space(3)))
#define AS1 __attribute__((address_space(1)))
#define AS4 __attribute__((address_space(4)))
#define CONV_X0 \
  "LwX%=:\n\t" \
  "ds_read_b32 v232, %12\n\t" \
  "s_waitcnt lgkmcnt(0)\n\t" \
  "v_readfirstlane_b32 %8, v232\n\t" \
  "s_cmp_lt_u32 %8, 4\n\t" \
  "s_cbranch_scc1 LwX%=\n\t" \
  "ds_read_b64 v[96:97], %4 offset:8\n\t" \
  "ds_read_b128 v[98:101], %4 offset:32\n\t" \
  "ds_read_b128 v[168:171], %6 offset:0\n\t" \
  "ds_read_b64 v[114:115], %5 offset:8\n\t" \
  "ds_read_b128 v[116:119], %5 offset:32\n\t" \
  "ds_read_b128 v[172:175], %6 offset:1024\n\t" \
  "ds_read_b64 v[132:133], %5 offset:1096\n\t" \
  "ds_read_b128 v[134:137], %5 offset:1120\n\t" \
  "ds_read_b128 v[176:179], %6 offset:2048\n\t" \
  "ds_read_b64 v[150:151], %5 offset:2184\n\t" \
  "ds_read_b128 v[152:155], %5 offset:2208\n\t" \
  "ds_read_b128 v[180:183], %6 offset:3072\n\t" \
  "ds_read_b128 v[102:105], %4 offset:48\n\t" \
  "ds_read_b128 v[184:187], %6 offset:16\n\t" \
  "ds_read_b128 v[120:123], %5 offset:48\n\t" \
  "ds_read_b128 v[188:191], %6 offset:1040\n\t" \
  "ds_read_b128 v[138:141], %5 offset:1136\n\t" \
  "ds_read_b128 v[192:195], %6 offset:2064\n\t" \
  "ds_read_b128 v[156:159], %5 offset:2224\n\t" \
  "ds_read_b128 v[196:199], %6 offset:3088\n\t" \
  "LcX%=:\n\t" \
  "s_waitcnt lgkmcnt(8)\n\t" \
  "v_mfma_f32_16x16x32_bf16 %0, v[96:99], v[168:171], %0\n\t" \
  "ds_read_b32 v232, %12 offset:4\n\t" \
  "ds_read_b128 v[106:109], %4 offset:64\n\t" \
  "v_mfma_f32_16x16x32_bf16 %1, v[98:101], v[168:171], %1\n\t" \
  "ds_read_b128 v[200:203], %6 offset:32\n\t" \
  "v_mfma_f32_16x16x32_bf16 %0, v[114:117], v[172:175], %0\n\t" \
  "ds_read_b128 v[124:127], %5 offset:64\n\t" \
  "v_mfma_f32_16x16x32_bf16 %1, v[116:119], v[172:175], %1\n\t" \
  "ds_read_b128 v[204:207], %6 offset:1056\n\t" \
  "v_mfma_f32_16x16x32_bf16 %0, v[132:135], v[176:179], %0\n\t" \
  "ds_read_b128 v[142:145], %5 offset:1152\n\t" \
  "v_mfma_f32_16x16x32_bf16 %1, v[134:137], v[176:179], %1\n\t" \
  "ds_read_b128 v[208:211], %6 offset:2080\n\t" \
  "v_mfma_f32_16x16x32_bf16 %0, v[150:153], v[180:183], %0\n\t" \
  "ds_read_b128 v[160:163], %5 offset:2240\n\t" \
  "v_mfma_f32_16x16x32_bf16 %1, v[152:155], v[180:183], %1\n\t" \
  "ds_read_b128 v[212:215], %6 offset:3104\n\t" \
  "v_mfma_f32_16x16x32_bf16 %2, v[96:99], v[180:183], %2\n\t" \
  "v_mfma_f32_16x16x32_bf16 %3, v[98:101], v[180:183], %3\n\t" \
  "s_cmp_lg_u32 %7, %11\n\t" \
  "s_cbranch_scc1 LsX%=\n\t" \
  "s_waitcnt vmcnt(0) lgkmcnt(8)\n\t" \
  "LpX%=:\n\t" \
  "v_readfirstlane_b32 %8, v232\n\t" \
  "s_cmp_ge_u32 %8, 4\n\t" \
  "s_cbranch_scc1 LqX%=\n\t" \
  "ds_read_b32 v232, %12 offset:4\n\t" \
  "s_waitcnt lgkmcnt(0)\n\t" \
  "s_branch LpX%=\n\t" \
  "LqX%=:\n\t" \
  "buffer_store_dwordx2 %15, %14, %16, 0 offen sc1\n\t" \
  "LsX%=:\n\t" \
  "s_waitcnt lgkmcnt(9)\n\t" \
  "v_mfma_f32_16x16x32_bf16 %0, v[100:103], v[184:187], %0\n\t" \
  "ds_read_b128 v[110:113], %4 offset:80\n\t" \
  "v_mfma_f32_16x16x32_bf16 %1, v[102:105], v[184:187], %1\n\t" \
  "ds_read_b128 v[216:219], %6 offset:48\n\t" \
  "v_mfma_f32_16x16x32_bf16 %0, v[118:121], v[188:191], %0\n\t" \
  "ds_read_b128 v[128:131], %5 offset:80\n\t" \
  "v_mfma_f32_16x16x32_bf16 %1, v[120:123], v[188:191], %1\n\t" \
  "ds_read_b128 v[220:223], %6 offset:1072\n\t" \
  "v_mfma_f32_16x16x32_bf16 %0, v[136:139], v[192:195], %0\n\t" \
  "ds_read_b128 v[146:149], %5 offset:1168\n\t" \
  "v_mfma_f32_16x16x32_bf16 %1, v[138:141], v[192:195], %1\n\t" \
  "ds_read_b128 v[224:227], %6 offset:2096\n\t" \
  "v_mfma_f32_16x16x32_bf16 %0, v[154:157], v[196:199], %0\n\t" \
  "ds_read_b128 v[164:167], %5 offset:2256\n\t" \
  "v_mfma_f32_16x16x32_bf16 %1, v[156:159], v[196:199], %1\n\t" \
  "ds_read_b128 v[228:231], %6 offset:3120\n\t" \
  "v_mfma_f32_16x16x32_bf16 %2, v[100:103], v[196:199], %2\n\t" \
  "v_mfma_f32_16x16x32_bf16 %3, v[102:105], v[196:199], %3\n\t" \
  "s_cmp_eq_u32 %7, 1\n\t" \
  "s_waitcnt lgkmcnt(8)\n\t" \
  "s_cbranch_scc1 LtX%=\n\t" \
  "v_mfma_f32_16x16x32_bf16 %0, v[104:107], v[200:203], %0\n\t" \
  "ds_read_b128 v[98:101], %4 offset:96\n\t" \
  "v_mfma_f32_16x16x32_bf16 %1, v[106:109], v[200:203], %1\n\t" \
  "ds_read_b128 v[168:171], %6 offset:64\n\t" \
  "v_mfma_f32_16x16x32_bf16 %0, v[122:125], v[204:207], %0\n\t" \
  "ds_read_b128 v[116:119], %5 offset:96\n\t" \
  "v_mfma_f32_16x16x32_bf16 %1, v[124:127], v[204:207], %1\n\t" \
  "ds_read_b128 v[172:175], %6 offset:1088\n\t" \
  "v_mfma_f32_16x16x32_bf16 %0, v[140:143], v[208:211], %0\n\t" \
  "ds_read_b128 v[134:137], %5 offset:1184\n\t" \
  "v_mfma_f32_16x16x32_bf16 %1, v[142:145], v[208:211], %1\n\t" \
  "ds_read_b128 v[176:179], %6 offset:2112\n\t" \
  "v_mfma_f32_16x16x32_bf16 %0, v[158:161], v[212:215], %0\n\t" \
  "ds_read_b128 v[152:155], %5 offset:2272\n\t" \
  "v_mfma_f32_16x16x32_bf16 %1, v[160:163], v[212:215], %1\n\t" \
  "ds_read_b128 v[180:183], %6 offset:3136\n\t" \
  "v_mfma_f32_16x16x32_bf16 %2, v[104:107], v[212:215], %2\n\t" \
  "v_mfma_f32_16x16x32_bf16 %3, v[106:109], v[212:215], %3\n\t" \
  "s_waitcnt lgkmcnt(8)\n\t" \
  "v_mfma_f32_16x16x32_bf16 %0, v[108:111], v[216:219], %0\n\t" \
  "ds_read_b128 v[102:105], %4 offset:112\n\t" \
  "v_mov_b64 v[96:97], v[112:113]\n\t" \
  "v_mfma_f32_16x16x32_bf16 %1, v[110:113], v[216:219], %1\n\t" \
  "ds_read_b128 v[184:187], %6 offset:80\n\t" \
  "v_mfma_f32_16x16x32_bf16 %0, v[126:129], v[220:223], %0\n\t" \
  "ds_read_b128 v[120:123], %5 offset:112\n\t" \
  "v_mov_b64 v[114:115], v[130:131]\n\t" \
  "v_mfma_f32_16x16x32_bf16 %1, v[128:131], v[220:223], %1\n\t" \
  "ds_read_b128 v[188:191], %6 offset:1104\n\t" \
  "v_mfma_f32_16x16x32_bf16 %0, v[144:147], v[224:227], %0\n\t" \
  "ds_read_b128 v[138:141], %5 offset:1200\n\t" \
  "v_mov_b64 v[132:133], v[148:149]\n\t" \
  "v_mfma_f32_16x16x32_bf16 %1, v[146:149], v[224:227], %1\n\t" \
  "ds_read_b128 v[192:195], %6 offset:2128\n\t" \
  "v_mfma_f32_16x16x32_bf16 %0, v[162:165], v[228:231], %0\n\t" \
  "ds_read_b128 v[156:159], %5 offset:2288\n\t" \
  "v_mov_b64 v[150:151], v[166:167]\n\t" \
  "v_mfma_f32_16x16x32_bf16 %1, v[164:167], v[228:231], %1\n\t" \
  "ds_read_b128 v[196:199], %6 offset:3152\n\t" \
  "v_add_u32 %6, 64, %6\n\t" \
  "v_mfma_f32_16x16x32_bf16 %2, v[108:111], v[228:231], %2\n\t" \
  "v_add_u32 %4, 64, %4\n\t" \
  "v_add_u32 %5, 64, %5\n\t" \
  "v_mfma_f32_16x16x32_bf16 %3, v[110:113], v[228:231], %3\n\t" \
  "s_sub_u32 %7, %7, 1\n\t" \
  "s_branch LcX%=\n\t" \
  "LtX%=:\n\t" \
  "v_mfma_f32_16x16x32_bf16 %0, v[104:107], v[200:203], %0\n\t" \
  "ds_read_b128 %9, %13 offset:0\n\t" \
  "v_mfma_f32_16x16x32_bf16 %1, v[106:109], v[200:203], %1\n\t" \
  "v_mfma_f32_16x16x32_bf16 %0, v[122:125], v[204:207], %0\n\t" \
  "ds_read_b128 %10, %13 offset:3072\n\t" \
  "v_mfma_f32_16x16x32_bf16 %1, v[124:127], v[204:207], %1\n\t" \
  "v_mfma_f32_16x16x32_bf16 %0, v[140:143], v[208:211], %0\n\t" \
  "v_mfma_f32_16x16x32_bf16 %1, v[142:145], v[208:211], %1\n\t" \
  "v_mfma_f32_16x16x32_bf16 %0, v[158:161], v[212:215], %0\n\t" \
  "v_mfma_f32_16x16x32_bf16 %1, v[160:163], v[212:215], %1\n\t" \
  "v_mfma_f32_16x16x32_bf16 %2, v[104:107], v[212:215], %2\n\t" \
  "v_mfma_f32_16x16x32_bf16 %3, v[106:109], v[212:215], %3\n\t" \
  "s_waitcnt lgkmcnt(0)\n\t" \
  "v_mfma_f32_16x16x32_bf16 %0, v[108:111], v[216:219], %0\n\t" \
  "v_mfma_f32_16x16x32_bf16 %1, v[110:113], v[216:219], %1\n\t" \
  "v_mfma_f32_16x16x32_bf16 %0, v[126:129], v[220:223], %0\n\t" \
  "v_mfma_f32_16x16x32_bf16 %1, v[128:131], v[220:223], %1\n\t" \
  "v_mfma_f32_16x16x32_bf16 %0, v[144:147], v[224:227], %0\n\t" \
  "v_mfma_f32_16x16x32_bf16 %1, v[146:149], v[224:227], %1\n\t" \
  "v_mfma_f32_16x16x32_bf16 %0, v[162:165], v[228:231], %0\n\t" \
  "v_mfma_f32_16x16x32_bf16 %1, v[164:167], v[228:231], %1\n\t" \
  "v_mfma_f32_16x16x32_bf16 %2, v[108:111], v[228:231], %2\n\t" \
  "v_mfma_f32_16x16x32_bf16 %3, v[110:113], v[228:231], %3\n\t" \
  "s_nop 7\n\t" \
  "s_nop 0\n\t"
#define CONV_Y0 \
  "LwY%=:\n\t" \
  "ds_read_b32 v232, %12\n\t" \
  "s_waitcnt lgkmcnt(0)\n\t" \
  "v_readfirstlane_b32 %8, v232\n\t" \
  "s_cmp_lt_u32 %8, 4\n\t" \
  "s_cbranch_scc1 LwY%=\n\t" \
  "ds_read_b64 v[96:97], %4 offset:8\n\t" \
  "ds_read_b128 v[98:101], %4 offset:32\n\t" \
  "ds_read_b128 v[168:171], %6 offset:1024\n\t" \
  "ds_read_b64 v[114:115], %5 offset:8\n\t" \
  "ds_read_b128 v[116:119], %5 offset:32\n\t" \
  "ds_read_b128 v[172:175], %6 offset:2048\n\t" \
  "ds_read_b64 v[132:133], %5 offset:1096\n\t" \
  "ds_read_b128 v[134:137], %5 offset:1120\n\t" \
  "ds_read_b128 v[176:179], %6 offset:3072\n\t" \
  "ds_read_b128 v[102:105], %4 offset:48\n\t" \
  "ds_read_b128 v[184:187], %6 offset:1040\n\t" \
  "ds_read_b128 v[120:123], %5 offset:48\n\t" \
  "ds_read_b128 v[188:191], %6 offset:2064\n\t" \
  "ds_read_b128 v[138:141], %5 offset:1136\n\t" \
  "ds_read_b128 v[192:195], %6 offset:3088\n\t" \
  "LcY%=:\n\t" \
  "s_waitcnt lgkmcnt(6)\n\t" \
  "v_mfma_f32_16x16x32_bf16 %0, v[96:99], v[168:171], %0\n\t" \
  "ds_read_b32 v232, %12 offset:4\n\t" \
  "ds_read_b128 v[106:109], %4 offset:64\n\t" \
  "v_mfma_f32_16x16x32_bf16 %1, v[98:101], v[168:171], %1\n\t" \
  "ds_read_b128 v[200:203], %6 offset:1056\n\t" \
  "v_mfma_f32_16x16x32_bf16 %0, v[114:117], v[172:175], %0\n\t" \
  "ds_read_b128 v[124:127], %5 offset:64\n\t" \
  "v_mfma_f32_16x16x32_bf16 %1, v[116:119], v[172:175], %1\n\t" \
  "ds_read_b128 v[204:207], %6 offset:2080\n\t" \
  "v_mfma_f32_16x16x32_bf16 %2, v[96:99], v[172:175], %2\n\t" \
  "v_mfma_f32_16x16x32_bf16 %3, v[98:101], v[172:175], %3\n\t" \
  "v_mfma_f32_16x16x32_bf16 %0, v[132:135], v[176:179], %0\n\t" \
  "ds_read_b128 v[142:145], %5 offset:1152\n\t" \
  "v_mfma_f32_16x16x32_bf16 %1, v[134:137], v[176:179], %1\n\t" \
  "ds_read_b128 v[208:211], %6 offset:3104\n\t" \
  "v_mfma_f32_16x16x32_bf16 %2, v[114:117], v[176:179], %2\n\t" \
  "v_mfma_f32_16x16x32_bf16 %3, v[116:119], v[176:179], %3\n\t" \
  "s_cmp_lg_u32 %7, %11\n\t" \
  "s_cbranch_scc1 LsY%=\n\t" \
  "s_waitcnt vmcnt(0) lgkmcnt(6)\n\t" \
  "LpY%=:\n\t" \
  "v_readfirstlane_b32 %8, v232\n\t" \
  "s_cmp_ge_u32 %8, 4\n\t" \
  "s_cbranch_scc1 LqY%=\n\t" \
  "ds_read_b32 v232, %12 offset:4\n\t" \
  "s_waitcnt lgkmcnt(0)\n\t" \
  "s_branch LpY%=\n\t" \
  "LqY%=:\n\t" \
  "buffer_store_dwordx2 %15, %14, %16, 0 offen sc1\n\t" \
  "LsY%=:\n\t" \
  "s_waitcnt lgkmcnt(7)\n\t" \
  "v_mfma_f32_16x16x32_bf16 %0, v[100:103], v[184:187], %0\n\t" \
  "ds_read_b128 v[110:113], %4 offset:80\n\t" \
  "v_mfma_f32_16x16x32_bf16 %1, v[102:105], v[184:187], %1\n\t" \
  "ds_read_b128 v[216:219], %6 offset:1072\n\t" \
  "v_mfma_f32_16x16x32_bf16 %0, v[118:121], v[188:191], %0\n\t" \
  "ds_read_b128 v[128:131], %5 offset:80\n\t" \
  "v_mfma_f32_16x16x32_bf16 %1, v[120:123], v[188:191], %1\n\t" \
  "ds_read_b128 v[220:223], %6 offset:2096\n\t" \
  "v_mfma_f32_16x16x32_bf16 %2, v[100:103], v[188:191], %2\n\t" \
  "v_mfma_f32_16x16x32_bf16 %3, v[102:105], v[188:191], %3\n\t" \
  "v_mfma_f32_16x16x32_bf16 %0, v[136:139], v[192:195], %0\n\t" \
  "ds_read_b128 v[146:149], %5 offset:1168\n\t" \
  "v_mfma_f32_16x16x32_bf16 %1, v[138:141], v[192:195], %1\n\t" \
  "ds_read_b128 v[224:227], %6 offset:3120\n\t" \
  "v_mfma_f32_16x16x32_bf16 %2, v[118:121], v[192:195], %2\n\t" \
  "v_mfma_f32_16x16x32_bf16 %3, v[120:123], v[192:195], %3\n\t" \
  "s_cmp_eq_u32 %7, 1\n\t" \
  "s_waitcnt lgkmcnt(6)\n\t" \
  "s_cbranch_scc1 LtY%=\n\t" \
  "v_mfma_f32_16x16x32_bf16 %0, v[104:107], v[200:203], %0\n\t" \
  "ds_read_b128 v[98:101], %4 offset:96\n\t" \
  "v_mfma_f32_16x16x32_bf16 %1, v[106:109], v[200:203], %1\n\t" \
  "ds_read_b128 v[168:171], %6 offset:1088\n\t" \
  "v_mfma_f32_16x16x32_bf16 %0, v[122:125], v[204:207], %0\n\t" \
  "ds_read_b128 v[116:119], %5 offset:96\n\t" \
  "v_mfma_f32_16x16x32_bf16 %1, v[124:127], v[204:207], %1\n\t" \
  "ds_read_b128 v[172:175], %6 offset:2112\n\t" \
  "v_mfma_f32_16x16x32_bf16 %2, v[104:107], v[204:207], %2\n\t" \
  "v_mfma_f32_16x16x32_bf16 %3, v[106:109], v[204:207], %3\n\t" \
  "v_mfma_f32_16x16x32_bf16 %0, v[140:143], v[208:211], %0\n\t" \
  "ds_read_b128 v[134:137], %5 offset:1184\n\t" \
  "v_mfma_f32_16x16x32_bf16 %1, v[142:145], v[208:211], %1\n\t" \
  "ds_read_b128 v[176:179], %6 offset:3136\n\t" \
  "v_mfma_f32_16x16x32_bf16 %2, v[122:125], v[208:211], %2\n\t" \
  "v_mfma_f32_16x16x32_bf16 %3, v[124:127], v[208:211], %3\n\t" \
  "s_waitcnt lgkmcnt(6)\n\t" \
  "v_mfma_f32_16x16x32_bf16 %0, v[108:111], v[216:219], %0\n\t" \
  "ds_read_b128 v[102:105], %4 offset:112\n\t" \
  "v_mov_b64 v[96:97], v[112:113]\n\t" \
  "v_mfma_f32_16x16x32_bf16 %1, v[110:113], v[216:219], %1\n\t" \
  "ds_read_b128 v[184:187], %6 offset:1104\n\t" \
  "v_mfma_f32_16x16x32_bf16 %0, v[126:129], v[220:223], %0\n\t" \
  "ds_read_b128 v[120:123], %5 offset:112\n\t" \
  "v_mov_b64 v[114:115], v[130:131]\n\t" \
  "v_mfma_f32_16x16x32_bf16 %1, v[128:131], v[220:223], %1\n\t" \
  "ds_read_b128 v[188:191], %6 offset:2128\n\t" \
  "v_mfma_f32_16x16x32_bf16 %2, v[108:111], v[220:223], %2\n\t" \
  "v_mfma_f32_16x16x32_bf16 %3, v[110:113], v[220:223], %3\n\t" \
  "v_mfma_f32_16x16x32_bf16 %0, v[144:147], v[224:227], %0\n\t" \
  "ds_read_b128 v[138:141], %5 offset:1200\n\t" \
  "v_mov_b64 v[132:133], v[148:149]\n\t" \
  "v_mfma_f32_16x16x32_bf16 %1, v[146:149], v[224:227], %1\n\t" \
  "ds_read_b128 v[192:195], %6 offset:3152\n\t" \
  "v_add_u32 %6, 64, %6\n\t" \
  "v_mfma_f32_16x16x32_bf16 %2, v[126:129], v[224:227], %2\n\t" \
  "v_add_u32 %4, 64, %4\n\t" \
  "v_add_u32 %5, 64, %5\n\t" \
  "v_mfma_f32_16x16x32_bf16 %3, v[128:131], v[224:227], %3\n\t" \
  "s_sub_u32 %7, %7, 1\n\t" \
  "s_branch LcY%=\n\t" \
  "LtY%=:\n\t" \
  "v_mfma_f32_16x16x32_bf16 %0, v[104:107], v[200:203], %0\n\t" \
  "ds_read_b128 %9, %13 offset:1024\n\t" \
  "v_mfma_f32_16x16x32_bf16 %1, v[106:109], v[200:203], %1\n\t" \
  "v_mfma_f32_16x16x32_bf16 %0, v[122:125], v[204:207], %0\n\t" \
  "ds_read_b128 %10, %13 offset:2048\n\t" \
  "v_mfma_f32_16x16x32_bf16 %1, v[124:127], v[204:207], %1\n\t" \
  "v_mfma_f32_16x16x32_bf16 %2, v[104:107], v[204:207], %2\n\t" \
  "v_mfma_f32_16x16x32_bf16 %3, v[106:109], v[204:207], %3\n\t" \
  "v_mfma_f32_16x16x32_bf16 %0, v[140:143], v[208:211], %0\n\t" \
  "v_mfma_f32_16x16x32_bf16 %1, v[142:145], v[208:211], %1\n\t" \
  "v_mfma_f32_16x16x32_bf16 %2, v[122:125], v[208:211], %2\n\t" \
  "v_mfma_f32_16x16x32_bf16 %3, v[124:127], v[208:211], %3\n\t" \
  "s_waitcnt lgkmcnt(0)\n\t" \
  "v_mfma_f32_16x16x32_bf16 %0, v[108:111], v[216:219], %0\n\t" \
  "v_mfma_f32_16x16x32_bf16 %1, v[110:113], v[216:219], %1\n\t" \
  "v_mfma_f32_16x16x32_bf16 %0, v[126:129], v[220:223], %0\n\t" \
  "v_mfma_f32_16x16x32_bf16 %1, v[128:131], v[220:223], %1\n\t" \
  "v_mfma_f32_16x16x32_bf16 %2, v[108:111], v[220:223], %2\n\t" \
  "v_mfma_f32_16x16x32_bf16 %3, v[110:113], v[220:223], %3\n\t" \
  "v_mfma_f32_16x16x32_bf16 %0, v[144:147], v[224:227], %0\n\t" \
  "v_mfma_f32_16x16x32_bf16 %1, v[146:149], v[224:227], %1\n\t" \
  "v_mfma_f32_16x16x32_bf16 %2, v[126:129], v[224:227], %2\n\t" \
  "v_mfma_f32_16x16x32_bf16 %3, v[128:131], v[224:227], %3\n\t" \
  "s_nop 7\n\t" \
  "s_nop 0\n\t"
#define CONV_X1 \
  "LwX%=:\n\t" \
  "ds_read_b32 v232, %12\n\t" \
  "s_waitcnt lgkmcnt(0)\n\t" \
  "v_readfirstlane_b32 %8, v232\n\t" \
  "s_cmp_lt_u32 %8, 4\n\t" \
  "s_cbranch_scc1 LwX%=\n\t" \
  "ds_read_b64 v[96:97], %4 offset:8\n\t" \
  "ds_read_b128 v[98:101], %4 offset:32\n\t" \
  "ds_read_b128 v[168:171], %6 offset:0\n\t" \
  "ds_read_b64 v[114:115], %5 offset:8\n\t" \
  "ds_read_b128 v[116:119], %5 offset:32\n\t" \
  "ds_read_b128 v[172:175], %6 offset:1024\n\t" \
  "ds_read_b64 v[132:133], %5 offset:1096\n\t" \
  "ds_read_b128 v[134:137], %5 offset:1120\n\t" \
  "ds_read_b128 v[176:179], %6 offset:2048\n\t" \
  "ds_read_b64 v[150:151], %5 offset:2184\n\t" \
  "ds_read_b128 v[152:155], %5 offset:2208\n\t" \
  "ds_read_b128 v[180:183], %6 offset:3072\n\t" \
  "ds_read_b128 v[102:105], %4 offset:48\n\t" \
  "ds_read_b128 v[184:187], %6 offset:16\n\t" \
  "ds_read_b128 v[120:123], %5 offset:48\n\t" \
  "ds_read_b128 v[188:191], %6 offset:1040\n\t" \
  "ds_read_b128 v[138:141], %5 offset:1136\n\t" \
  "ds_read_b128 v[192:195], %6 offset:2064\n\t" \
  "ds_read_b128 v[156:159], %5 offset:2224\n\t" \
  "ds_read_b128 v[196:199], %6 offset:3088\n\t" \
  "LcX%=:\n\t" \
  "s_waitcnt lgkmcnt(8)\n\t" \
  "v_mfma_f32_16x16x32_bf16 %0, v[96:99], v[168:171], %0\n\t" \
  "ds_read_b128 v[106:109], %4 offset:64\n\t" \
  "v_mfma_f32_16x16x32_bf16 %1, v[98:101], v[168:171], %1\n\t" \
  "ds_read_b128 v[200:203], %6 offset:32\n\t" \
  "v_mfma_f32_16x16x32_bf16 %0, v[114:117], v[172:175], %0\n\t" \
  "ds_read_b128 v[124:127], %5 offset:64\n\t" \
  "v_mfma_f32_16x16x32_bf16 %1, v[116:119], v[172:175], %1\n\t" \
  "ds_read_b128 v[204:207], %6 offset:1056\n\t" \
  "v_mfma_f32_16x16x32_bf16 %0, v[132:135], v[176:179], %0\n\t" \
  "ds_read_b128 v[142:145], %5 offset:1152\n\t" \
  "v_mfma_f32_16x16x32_bf16 %1, v[134:137], v[176:179], %1\n\t" \
  "ds_read_b128 v[208:211], %6 offset:2080\n\t" \
  "v_mfma_f32_16x16x32_bf16 %0, v[150:153], v[180:183], %0\n\t" \
  "ds_read_b128 v[160:163], %5 offset:2240\n\t" \
  "v_mfma_f32_16x16x32_bf16 %1, v[152:155], v[180:183], %1\n\t" \
  "ds_read_b128 v[212:215], %6 offset:3104\n\t" \
  "v_mfma_f32_16x16x32_bf16 %2, v[96:99], v[180:183], %2\n\t" \
  "v_mfma_f32_16x16x32_bf16 %3, v[98:101], v[180:183], %3\n\t" \
  "s_waitcnt lgkmcnt(8)\n\t" \
  "v_mfma_f32_16x16x32_bf16 %0, v[100:103], v[184:187], %0\n\t" \
  "ds_read_b32 v232, %12 offset:4\n\t" \
  "ds_read_b128 v[110:113], %4 offset:80\n\t" \
  "v_mfma_f32_16x16x32_bf16 %1, v[102:105], v[184:187], %1\n\t" \
  "ds_read_b128 v[216:219], %6 offset:48\n\t" \
  "v_mfma_f32_16x16x32_bf16 %0, v[118:121], v[188:191], %0\n\t" \
  "ds_read_b128 v[128:131], %5 offset:80\n\t" \
  "v_mfma_f32_16x16x32_bf16 %1, v[120:123], v[188:191], %1\n\t" \
  "ds_read_b128 v[220:223], %6 offset:1072\n\t" \
  "v_mfma_f32_16x16x32_bf16 %0, v[136:139], v[192:195], %0\n\t" \
  "ds_read_b128 v[146:149], %5 offset:1168\n\t" \
  "v_mfma_f32_16x16x32_bf16 %1, v[138:141], v[192:195], %1\n\t" \
  "ds_read_b128 v[224:227], %6 offset:2096\n\t" \
  "v_mfma_f32_16x16x32_bf16 %0, v[154:157], v[196:199], %0\n\t" \
  "ds_read_b128 v[164:167], %5 offset:2256\n\t" \
  "v_mfma_f32_16x16x32_bf16 %1, v[156:159], v[196:199], %1\n\t" \
  "ds_read_b128 v[228:231], %6 offset:3120\n\t" \
  "v_mfma_f32_16x16x32_bf16 %2, v[100:103], v[196:199], %2\n\t" \
  "v_mfma_f32_16x16x32_bf16 %3, v[102:105], v[196:199], %3\n\t" \
  "s_cmp_lg_u32 %7, %11\n\t" \
  "s_cbranch_scc1 LsX%=\n\t" \
  "s_waitcnt vmcnt(0) lgkmcnt(8)\n\t" \
  "LpX%=:\n\t" \
  "v_readfirstlane_b32 %8, v232\n\t" \
  "s_cmp_ge_u32 %8, 4\n\t" \
  "s_cbranch_scc1 LqX%=\n\t" \
  "ds_read_b32 v232, %12 offset:4\n\t" \
  "s_waitcnt lgkmcnt(0)\n\t" \
  "s_branch LpX%=\n\t" \
  "LqX%=:\n\t" \
  "buffer_store_dwordx2 %15, %14, %16, 0 offen sc1\n\t" \
  "LsX%=:\n\t" \
  "s_cmp_eq_u32 %7, 1\n\t" \
  "s_waitcnt lgkmcnt(9)\n\t" \
  "s_cbranch_scc1 LtX%=\n\t" \
  "v_mfma_f32_16x16x32_bf16 %0, v[104:107], v[200:203], %0\n\t" \
  "ds_read_b128 v[98:101], %4 offset:96\n\t" \
  "v_mfma_f32_16x16x32_bf16 %1, v[106:109], v[200:203], %1\n\t" \
  "ds_read_b128 v[168:171], %6 offset:64\n\t" \
  "v_mfma_f32_16x16x32_bf16 %0, v[122:125], v[204:207], %0\n\t" \
  "ds_read_b128 v[116:119], %5 offset:96\n\t" \
  "v_mfma_f32_16x16x32_bf16 %1, v[124:127], v[204:207], %1\n\t" \
  "ds_read_b128 v[172:175], %6 offset:1088\n\t" \
  "v_mfma_f32_16x16x32_bf16 %0, v[140:143], v[208:211], %0\n\t" \
  "ds_read_b128 v[134:137], %5 offset:1184\n\t" \
  "v_mfma_f32_16x16x32_bf16 %1, v[142:145], v[208:211], %1\n\t" \
  "ds_read_b128 v[176:179], %6 offset:2112\n\t" \
  "v_mfma_f32_16x16x32_bf16 %0, v[158:161], v[212:215], %0\n\t" \
  "ds_read_b128 v[152:155], %5 offset:2272\n\t" \
  "v_mfma_f32_16x16x32_bf16 %1, v[160:163], v[212:215], %1\n\t" \
  "ds_read_b128 v[180:183], %6 offset:3136\n\t" \
  "v_mfma_f32_16x16x32_bf16 %2, v[104:107], v[212:215], %2\n\t" \
  "v_mfma_f32_16x16x32_bf16 %3, v[106:109], v[212:215], %3\n\t" \
  "s_waitcnt lgkmcnt(8)\n\t" \
  "v_mfma_f32_16x16x32_bf16 %0, v[108:111], v[216:219], %0\n\t" \
  "ds_read_b128 v[102:105], %4 offset:112\n\t" \
  "v_mov_b64 v[96:97], v[112:113]\n\t" \
  "v_mfma_f32_16x16x32_bf16 %1, v[110:113], v[216:219], %1\n\t" \
  "ds_read_b128 v[184:187], %6 offset:80\n\t" \
  "v_mfma_f32_16x16x32_bf16 %0, v[126:129], v[220:223], %0\n\t" \
  "ds_read_b128 v[120:123], %5 offset:112\n\t" \
  "v_mov_b64 v[114:115], v[130:131]\n\t" \
  "v_mfma_f32_16x16x32_bf16 %1, v[128:131], v[220:223], %1\n\t" \
  "ds_read_b128 v[188:191], %6 offset:1104\n\t" \
  "v_mfma_f32_16x16x32_bf16 %0, v[144:147], v[224:227], %0\n\t" \
  "ds_read_b128 v[138:141], %5 offset:1200\n\t" \
  "v_mov_b64 v[132:133], v[148:149]\n\t" \
  "v_mfma_f32_16x16x32_bf16 %1, v[146:149], v[224:227], %1\n\t" \
  "ds_read_b128 v[192:195], %6 offset:2128\n\t" \
  "v_mfma_f32_16x16x32_bf16 %0, v[162:165], v[228:231], %0\n\t" \
  "ds_read_b128 v[156:159], %5 offset:2288\n\t" \
  "v_mov_b64 v[150:151], v[166:167]\n\t" \
  "v_mfma_f32_16x16x32_bf16 %1, v[164:167], v[228:231], %1\n\t" \
  "ds_read_b128 v[196:199], %6 offset:3152\n\t" \
  "v_add_u32 %6, 64, %6\n\t" \
  "v_mfma_f32_16x16x32_bf16 %2, v[108:111], v[228:231], %2\n\t" \
  "v_add_u32 %4, 64, %4\n\t" \
  "v_add_u32 %5, 64, %5\n\t" \
  "v_mfma_f32_16x16x32_bf16 %3, v[110:113], v[228:231], %3\n\t" \
  "s_sub_u32 %7, %7, 1\n\t" \
  "s_branch LcX%=\n\t" \
  "LtX%=:\n\t" \
  "v_mfma_f32_16x16x32_bf16 %0, v[104:107], v[200:203], %0\n\t" \
  "ds_read_b128 %9, %13 offset:0\n\t" \
  "v_mfma_f32_16x16x32_bf16 %1, v[106:109], v[200:203], %1\n\t" \
  "v_mfma_f32_16x16x32_bf16 %0, v[122:125], v[204:207], %0\n\t" \
  "ds_read_b128 %10, %13 offset:3072\n\t" \
  "v_mfma_f32_16x16x32_bf16 %1, v[124:127], v[204:207], %1\n\t" \
  "v_mfma_f32_16x16x32_bf16 %0, v[140:143], v[208:211], %0\n\t" \
  "v_mfma_f32_16x16x32_bf16 %1, v[142:145], v[208:211], %1\n\t" \
  "v_mfma_f32_16x16x32_bf16 %0, v[158:161], v[212:215], %0\n\t" \
  "v_mfma_f32_16x16x32_bf16 %1, v[160:163], v[212:215], %1\n\t" \
  "v_mfma_f32_16x16x32_bf16 %2, v[104:107], v[212:215], %2\n\t" \
  "v_mfma_f32_16x16x32_bf16 %3, v[106:109], v[212:215], %3\n\t" \
  "s_waitcnt lgkmcnt(0)\n\t" \
  "v_mfma_f32_16x16x32_bf16 %0, v[108:111], v[216:219], %0\n\t" \
  "v_mfma_f32_16x16x32_bf16 %1, v[110:113], v[216:219], %1\n\t" \
  "v_mfma_f32_16x16x32_bf16 %0, v[126:129], v[220:223], %0\n\t" \
  "v_mfma_f32_16x16x32_bf16 %1, v[128:131], v[220:223], %1\n\t" \
  "v_mfma_f32_16x16x32_bf16 %0, v[144:147], v[224:227], %0\n\t" \
  "v_mfma_f32_16x16x32_bf16 %1, v[146:149], v[224:227], %1\n\t" \
  "v_mfma_f32_16x16x32_bf16 %0, v[162:165], v[228:231], %0\n\t" \
  "v_mfma_f32_16x16x32_bf16 %1, v[164:167], v[228:231], %1\n\t" \
  "v_mfma_f32_16x16x32_bf16 %2, v[108:111], v[228:231], %2\n\t" \
  "v_mfma_f32_16x16x32_bf16 %3, v[110:113], v[228:231], %3\n\t" \
  "s_nop 7\n\t" \
  "s_nop 0\n\t"
#define CONV_Y1 \
  "LwY%=:\n\t" \
  "ds_read_b32 v232, %12\n\t" \
  "s_waitcnt lgkmcnt(0)\n\t" \
  "v_readfirstlane_b32 %8, v232\n\t" \
  "s_cmp_lt_u32 %8, 4\n\t" \
  "s_cbranch_scc1 LwY%=\n\t" \
  "ds_read_b64 v[96:97], %4 offset:8\n\t" \
  "ds_read_b128 v[98:101], %4 offset:32\n\t" \
  "ds_read_b128 v[168:171], %6 offset:1024\n\t" \
  "ds_read_b64 v[114:115], %5 offset:8\n\t" \
  "ds_read_b128 v[116:119], %5 offset:32\n\t" \
  "ds_read_b128 v[172:175], %6 offset:2048\n\t" \
  "ds_read_b64 v[132:133], %5 offset:1096\n\t" \
  "ds_read_b128 v[134:137], %5 offset:1120\n\t" \
  "ds_read_b128 v[176:179], %6 offset:3072\n\t" \
  "ds_read_b128 v[102:105], %4 offset:48\n\t" \
  "ds_read_b128 v[184:187], %6 offset:1040\n\t" \
  "ds_read_b128 v[120:123], %5 offset:48\n\t" \
  "ds_read_b128 v[188:191], %6 offset:2064\n\t" \
  "ds_read_b128 v[138:141], %5 offset:1136\n\t" \
  "ds_read_b128 v[192:195], %6 offset:3088\n\t" \
  "LcY%=:\n\t" \
  "s_waitcnt lgkmcnt(6)\n\t" \
  "v_mfma_f32_16x16x32_bf16 %0, v[96:99], v[168:171], %0\n\t" \
  "ds_read_b128 v[106:109], %4 offset:64\n\t" \
  "v_mfma_f32_16x16x32_bf16 %1, v[98:101], v[168:171], %1\n\t" \
  "ds_read_b128 v[200:203], %6 offset:1056\n\t" \
  "v_mfma_f32_16x16x32_bf16 %0, v[114:117], v[172:175], %0\n\t" \
  "ds_read_b128 v[124:127], %5 offset:64\n\t" \
  "v_mfma_f32_16x16x32_bf16 %1, v[116:119], v[172:175], %1\n\t" \
  "ds_read_b128 v[204:207], %6 offset:2080\n\t" \
  "v_mfma_f32_16x16x32_bf16 %2, v[96:99], v[172:175], %2\n\t" \
  "v_mfma_f32_16x16x32_bf16 %3, v[98:101], v[172:175], %3\n\t" \
  "v_mfma_f32_16x16x32_bf16 %0, v[132:135], v[176:179], %0\n\t" \
  "ds_read_b128 v[142:145], %5 offset:1152\n\t" \
  "v_mfma_f32_16x16x32_bf16 %1, v[134:137], v[176:179], %1\n\t" \
  "ds_read_b128 v[208:211], %6 offset:3104\n\t" \
  "v_mfma_f32_16x16x32_bf16 %2, v[114:117], v[176:179], %2\n\t" \
  "v_mfma_f32_16x16x32_bf16 %3, v[116:119], v[176:179], %3\n\t" \
  "s_waitcnt lgkmcnt(6)\n\t" \
  "v_mfma_f32_16x16x32_bf16 %0, v[100:103], v[184:187], %0\n\t" \
  "ds_read_b32 v232, %12 offset:4\n\t" \
  "ds_read_b128 v[110:113], %4 offset:80\n\t" \
  "v_mfma_f32_16x16x32_bf16 %1, v[102:105], v[184:187], %1\n\t" \
  "ds_read_b128 v[216:219], %6 offset:1072\n\t" \
  "v_mfma_f32_16x16x32_bf16 %0, v[118:121], v[188:191], %0\n\t" \
  "ds_read_b128 v[128:131], %5 offset:80\n\t" \
  "v_mfma_f32_16x16x32_bf16 %1, v[120:123], v[188:191], %1\n\t" \
  "ds_read_b128 v[220:223], %6 offset:2096\n\t" \
  "v_mfma_f32_16x16x32_bf16 %2, v[100:103], v[188:191], %2\n\t" \
  "v_mfma_f32_16x16x32_bf16 %3, v[102:105], v[188:191], %3\n\t" \
  "v_mfma_f32_16x16x32_bf16 %0, v[136:139], v[192:195], %0\n\t" \
  "ds_read_b128 v[146:149], %5 offset:1168\n\t" \
  "v_mfma_f32_16x16x32_bf16 %1, v[138:141], v[192:195], %1\n\t" \
  "ds_read_b128 v[224:227], %6 offset:3120\n\t" \
  "v_mfma_f32_16x16x32_bf16 %2, v[118:121], v[192:195], %2\n\t" \
  "v_mfma_f32_16x16x32_bf16 %3, v[120:123], v[192:195], %3\n\t" \
  "s_cmp_lg_u32 %7, %11\n\t" \
  "s_cbranch_scc1 LsY%=\n\t" \
  "s_waitcnt vmcnt(0) lgkmcnt(6)\n\t" \
  "LpY%=:\n\t" \
  "v_readfirstlane_b32 %8, v232\n\t" \
  "s_cmp_ge_u32 %8, 4\n\t" \
  "s_cbranch_scc1 LqY%=\n\t" \
  "ds_read_b32 v232, %12 offset:4\n\t" \
  "s_waitcnt lgkmcnt(0)\n\t" \
  "s_branch LpY%=\n\t" \
  "LqY%=:\n\t" \
  "buffer_store_dwordx2 %15, %14, %16, 0 offen sc1\n\t" \
  "LsY%=:\n\t" \
  "s_cmp_eq_u32 %7, 1\n\t" \
  "s_waitcnt lgkmcnt(7)\n\t" \
  "s_cbranch_scc1 LtY%=\n\t" \
  "v_mfma_f32_16x16x32_bf16 %0, v[104:107], v[200:203], %0\n\t" \
  "ds_read_b128 v[98:101], %4 offset:96\n\t" \
  "v_mfma_f32_16x16x32_bf16 %1, v[106:109], v[200:203], %1\n\t" \
  "ds_read_b128 v[168:171], %6 offset:1088\n\t" \
  "v_mfma_f32_16x16x32_bf16 %0, v[122:125], v[204:207], %0\n\t" \
  "ds_read_b128 v[116:119], %5 offset:96\n\t" \
  "v_mfma_f32_16x16x32_bf16 %1, v[124:127], v[204:207], %1\n\t" \
  "ds_read_b128 v[172:175], %6 offset:2112\n\t" \
  "v_mfma_f32_16x16x32_bf16 %2, v[104:107], v[204:207], %2\n\t" \
  "v_mfma_f32_16x16x32_bf16 %3, v[106:109], v[204:207], %3\n\t" \
  "v_mfma_f32_16x16x32_bf16 %0, v[140:143], v[208:211], %0\n\t" \
  "ds_read_b128 v[134:137], %5 offset:1184\n\t" \
  "v_mfma_f32_16x16x32_bf16 %1, v[142:145], v[208:211], %1\n\t" \
  "ds_read_b128 v[176:179], %6 offset:3136\n\t" \
  "v_mfma_f32_16x16x32_bf16 %2, v[122:125], v[208:211], %2\n\t" \
  "v_mfma_f32_16x16x32_bf16 %3, v[124:127], v[208:211], %3\n\t" \
  "s_waitcnt lgkmcnt(6)\n\t" \
  "v_mfma_f32_16x16x32_bf16 %0, v[108:111], v[216:219], %0\n\t" \
  "ds_read_b128 v[102:105], %4 offset:112\n\t" \
  "v_mov_b64 v[96:97], v[112:113]\n\t" \
  "v_mfma_f32_16x16x32_bf16 %1, v[110:113], v[216:219], %1\n\t" \
  "ds_read_b128 v[184:187], %6 offset:1104\n\t" \
  "v_mfma_f32_16x16x32_bf16 %0, v[126:129], v[220:223], %0\n\t" \
  "ds_read_b128 v[120:123], %5 offset:112\n\t" \
  "v_mov_b64 v[114:115], v[130:131]\n\t" \
  "v_mfma_f32_16x16x32_bf16 %1, v[128:131], v[220:223], %1\n\t" \
  "ds_read_b128 v[188:191], %6 offset:2128\n\t" \
  "v_mfma_f32_16x16x32_bf16 %2, v[108:111], v[220:223], %2\n\t" \
  "v_mfma_f32_16x16x32_bf16 %3, v[110:113], v[220:223], %3\n\t" \
  "v_mfma_f32_16x16x32_bf16 %0, v[144:147], v[224:227], %0\n\t" \
  "ds_read_b128 v[138:141], %5 offset:1200\n\t" \
  "v_mov_b64 v[132:133], v[148:149]\n\t" \
  "v_mfma_f32_16x16x32_bf16 %1, v[146:149], v[224:227], %1\n\t" \
  "ds_read_b128 v[192:195], %6 offset:3152\n\t" \
  "v_add_u32 %6, 64, %6\n\t" \
  "v_mfma_f32_16x16x32_bf16 %2, v[126:129], v[224:227], %2\n\t" \
  "v_add_u32 %4, 64, %4\n\t" \
  "v_add_u32 %5, 64, %5\n\t" \
  "v_mfma_f32_16x16x32_bf16 %3, v[128:131], v[224:227], %3\n\t" \
  "s_sub_u32 %7, %7, 1\n\t" \
  "s_branch LcY%=\n\t" \
  "LtY%=:\n\t" \
  "v_mfma_f32_16x16x32_bf16 %0, v[104:107], v[200:203], %0\n\t" \
  "ds_read_b128 %9, %13 offset:1024\n\t" \
  "v_mfma_f32_16x16x32_bf16 %1, v[106:109], v[200:203], %1\n\t" \
  "v_mfma_f32_16x16x32_bf16 %0, v[122:125], v[204:207], %0\n\t" \
  "ds_read_b128 %10, %13 offset:2048\n\t" \
  "v_mfma_f32_16x16x32_bf16 %1, v[124:127], v[204:207], %1\n\t" \
  "v_mfma_f32_16x16x32_bf16 %2, v[104:107], v[204:207], %2\n\t" \
  "v_mfma_f32_16x16x32_bf16 %3, v[106:109], v[204:207], %3\n\t" \
  "v_mfma_f32_16x16x32_bf16 %0, v[140:143], v[208:211], %0\n\t" \
  "v_mfma_f32_16x16x32_bf16 %1, v[142:145], v[208:211], %1\n\t" \
  "v_mfma_f32_16x16x32_bf16 %2, v[122:125], v[208:211], %2\n\t" \
  "v_mfma_f32_16x16x32_bf16 %3, v[124:127], v[208:211], %3\n\t" \
  "s_waitcnt lgkmcnt(0)\n\t" \
  "v_mfma_f32_16x16x32_bf16 %0, v[108:111], v[216:219], %0\n\t" \
  "v_mfma_f32_16x16x32_bf16 %1, v[110:113], v[216:219], %1\n\t" \
  "v_mfma_f32_16x16x32_bf16 %0, v[126:129], v[220:223], %0\n\t" \
  "v_mfma_f32_16x16x32_bf16 %1, v[128:131], v[220:223], %1\n\t" \
  "v_mfma_f32_16x16x32_bf16 %2, v[108:111], v[220:223], %2\n\t" \
  "v_mfma_f32_16x16x32_bf16 %3, v[110:113], v[220:223], %3\n\t" \
  "v_mfma_f32_16x16x32_bf16 %0, v[144:147], v[224:227], %0\n\t" \
  "v_mfma_f32_16x16x32_bf16 %1, v[146:149], v[224:227], %1\n\t" \
  "v_mfma_f32_16x16x32_bf16 %2, v[126:129], v[224:227], %2\n\t" \
  "v_mfma_f32_16x16x32_bf16 %3, v[128:131], v[224:227], %3\n\t" \
  "s_nop 7\n\t" \
  "s_nop 0\n\t"
#define CONV_X2 \
  "LwX%=:\n\t" \
  "ds_read_b32 v232, %12\n\t" \
  "s_waitcnt lgkmcnt(0)\n\t" \
  "v_readfirstlane_b32 %8, v232\n\t" \
  "s_cmp_lt_u32 %8, 4\n\t" \
  "s_cbranch_scc1 LwX%=\n\t" \
  "ds_read_b64 v[96:97], %4 offset:8\n\t" \
  "ds_read_b128 v[98:101], %4 offset:32\n\t" \
  "ds_read_b128 v[168:171], %6 offset:0\n\t" \
  "ds_read_b64 v[114:115], %5 offset:8\n\t" \
  "ds_read_b128 v[116:119], %5 offset:32\n\t" \
  "ds_read_b128 v[172:175], %6 offset:1024\n\t" \
  "ds_read_b64 v[132:133], %5 offset:1096\n\t" \
  "ds_read_b128 v[134:137], %5 offset:1120\n\t" \
  "ds_read_b128 v[176:179], %6 offset:2048\n\t" \
  "ds_read_b64 v[150:151], %5 offset:2184\n\t" \
  "ds_read_b128 v[152:155], %5 offset:2208\n\t" \
  "ds_read_b128 v[180:183], %6 offset:3072\n\t" \
  "ds_read_b128 v[102:105], %4 offset:48\n\t" \
  "ds_read_b128 v[184:187], %6 offset:16\n\t" \
  "ds_read_b128 v[120:123], %5 offset:48\n\t" \
  "ds_read_b128 v[188:191], %6 offset:1040\n\t" \
  "ds_read_b128 v[138:141], %5 offset:1136\n\t" \
  "ds_read_b128 v[192:195], %6 offset:2064\n\t" \
  "ds_read_b128 v[156:159], %5 offset:2224\n\t" \
  "ds_read_b128 v[196:199], %6 offset:3088\n\t" \
  "LcX%=:\n\t" \
  "s_waitcnt lgkmcnt(8)\n\t" \
  "v_mfma_f32_16x16x32_bf16 %0, v[96:99], v[168:171], %0\n\t" \
  "ds_read_b128 v[106:109], %4 offset:64\n\t" \
  "v_mfma_f32_16x16x32_bf16 %1, v[98:101], v[168:171], %1\n\t" \
  "ds_read_b128 v[200:203], %6 offset:32\n\t" \
  "v_mfma_f32_16x16x32_bf16 %0, v[114:117], v[172:175], %0\n\t" \
  "ds_read_b128 v[124:127], %5 offset:64\n\t" \
  "v_mfma_f32_16x16x32_bf16 %1, v[116:119], v[172:175], %1\n\t" \
  "ds_read_b128 v[204:207], %6 offset:1056\n\t" \
  "v_mfma_f32_16x16x32_bf16 %0, v[132:135], v[176:179], %0\n\t" \
  "ds_read_b128 v[142:145], %5 offset:1152\n\t" \
  "v_mfma_f32_16x16x32_bf16 %1, v[134:137], v[176:179], %1\n\t" \
  "ds_read_b128 v[208:211], %6 offset:2080\n\t" \
  "v_mfma_f32_16x16x32_bf16 %0, v[150:153], v[180:183], %0\n\t" \
  "ds_read_b128 v[160:163], %5 offset:2240\n\t" \
  "v_mfma_f32_16x16x32_bf16 %1, v[152:155], v[180:183], %1\n\t" \
  "ds_read_b128 v[212:215], %6 offset:3104\n\t" \
  "v_mfma_f32_16x16x32_bf16 %2, v[96:99], v[180:183], %2\n\t" \
  "v_mfma_f32_16x16x32_bf16 %3, v[98:101], v[180:183], %3\n\t" \
  "s_waitcnt lgkmcnt(8)\n\t" \
  "v_mfma_f32_16x16x32_bf16 %0, v[100:103], v[184:187], %0\n\t" \
  "ds_read_b128 v[110:113], %4 offset:80\n\t" \
  "v_mfma_f32_16x16x32_bf16 %1, v[102:105], v[184:187], %1\n\t" \
  "ds_read_b128 v[216:219], %6 offset:48\n\t" \
  "v_mfma_f32_16x16x32_bf16 %0, v[118:121], v[188:191], %0\n\t" \
  "ds_read_b128 v[128:131], %5 offset:80\n\t" \
  "v_mfma_f32_16x16x32_bf16 %1, v[120:123], v[188:191], %1\n\t" \
  "ds_read_b128 v[220:223], %6 offset:1072\n\t" \
  "v_mfma_f32_16x16x32_bf16 %0, v[136:139], v[192:195], %0\n\t" \
  "ds_read_b128 v[146:149], %5 offset:1168\n\t" \
  "v_mfma_f32_16x16x32_bf16 %1, v[138:141], v[192:195], %1\n\t" \
  "ds_read_b128 v[224:227], %6 offset:2096\n\t" \
  "v_mfma_f32_16x16x32_bf16 %0, v[154:157], v[196:199], %0\n\t" \
  "ds_read_b128 v[164:167], %5 offset:2256\n\t" \
  "v_mfma_f32_16x16x32_bf16 %1, v[156:159], v[196:199], %1\n\t" \
  "ds_read_b128 v[228:231], %6 offset:3120\n\t" \
  "v_mfma_f32_16x16x32_bf16 %2, v[100:103], v[196:199], %2\n\t" \
  "v_mfma_f32_16x16x32_bf16 %3, v[102:105], v[196:199], %3\n\t" \
  "s_cmp_eq_u32 %7, 1\n\t" \
  "s_waitcnt lgkmcnt(8)\n\t" \
  "s_cbranch_scc1 LtX%=\n\t" \
  "v_mfma_f32_16x16x32_bf16 %0, v[104:107], v[200:203], %0\n\t" \
  "ds_read_b32 v232, %12 offset:4\n\t" \
  "ds_read_b128 v[98:101], %4 offset:96\n\t" \
  "v_mfma_f32_16x16x32_bf16 %1, v[106:109], v[200:203], %1\n\t" \
  "ds_read_b128 v[168:171], %6 offset:64\n\t" \
  "v_mfma_f32_16x16x32_bf16 %0, v[122:125], v[204:207], %0\n\t" \
  "ds_read_b128 v[116:119], %5 offset:96\n\t" \
  "v_mfma_f32_16x16x32_bf16 %1, v[124:127], v[204:207], %1\n\t" \
  "ds_read_b128 v[172:175], %6 offset:1088\n\t" \
  "v_mfma_f32_16x16x32_bf16 %0, v[140:143], v[208:211], %0\n\t" \
  "ds_read_b128 v[134:137], %5 offset:1184\n\t" \
  "v_mfma_f32_16x16x32_bf16 %1, v[142:145], v[208:211], %1\n\t" \
  "ds_read_b128 v[176:179], %6 offset:2112\n\t" \
  "v_mfma_f32_16x16x32_bf16 %0, v[158:161], v[212:215], %0\n\t" \
  "ds_read_b128 v[152:155], %5 offset:2272\n\t" \
  "v_mfma_f32_16x16x32_bf16 %1, v[160:163], v[212:215], %1\n\t" \
  "ds_read_b128 v[180:183], %6 offset:3136\n\t" \
  "v_mfma_f32_16x16x32_bf16 %2, v[104:107], v[212:215], %2\n\t" \
  "v_mfma_f32_16x16x32_bf16 %3, v[106:109], v[212:215], %3\n\t" \
  "s_cmp_lg_u32 %7, %11\n\t" \
  "s_cbranch_scc1 LsX%=\n\t" \
  "s_waitcnt vmcnt(0) lgkmcnt(8)\n\t" \
  "LpX%=:\n\t" \
  "v_readfirstlane_b32 %8, v232\n\t" \
  "s_cmp_ge_u32 %8, 4\n\t" \
  "s_cbranch_scc1 LqX%=\n\t" \
  "ds_read_b32 v232, %12 offset:4\n\t" \
  "s_waitcnt lgkmcnt(0)\n\t" \
  "s_branch LpX%=\n\t" \
  "LqX%=:\n\t" \
  "buffer_store_dwordx2 %15, %14, %16, 0 offen sc1\n\t" \
  "LsX%=:\n\t" \
  "s_waitcnt lgkmcnt(9)\n\t" \
  "v_mfma_f32_16x16x32_bf16 %0, v[108:111], v[216:219], %0\n\t" \
  "ds_read_b128 v[102:105], %4 offset:112\n\t" \
  "v_mov_b64 v[96:97], v[112:113]\n\t" \
  "v_mfma_f32_16x16x32_bf16 %1, v[110:113], v[216:219], %1\n\t" \
  "ds_read_b128 v[184:187], %6 offset:80\n\t" \
  "v_mfma_f32_16x16x32_bf16 %0, v[126:129], v[220:223], %0\n\t" \
  "ds_read_b128 v[120:123], %5 offset:112\n\t" \
  "v_mov_b64 v[114:115], v[130:131]\n\t" \
  "v_mfma_f32_16x16x32_bf16 %1, v[128:131], v[220:223], %1\n\t" \
  "ds_read_b128 v[188:191], %6 offset:1104\n\t" \
  "v_mfma_f32_16x16x32_bf16 %0, v[144:147], v[224:227], %0\n\t" \
  "ds_read_b128 v[138:141], %5 offset:1200\n\t" \
  "v_mov_b64 v[132:133], v[148:149]\n\t" \
  "v_mfma_f32_16x16x32_bf16 %1, v[146:149], v[224:227], %1\n\t" \
  "ds_read_b128 v[192:195], %6 offset:2128\n\t" \
  "v_mfma_f32_16x16x32_bf16 %0, v[162:165], v[228:231], %0\n\t" \
  "ds_read_b128 v[156:159], %5 offset:2288\n\t" \
  "v_mov_b64 v[150:151], v[166:167]\n\t" \
  "v_mfma_f32_16x16x32_bf16 %1, v[164:167], v[228:231], %1\n\t" \
  "ds_read_b128 v[196:199], %6 offset:3152\n\t" \
  "v_add_u32 %6, 64, %6\n\t" \
  "v_mfma_f32_16x16x32_bf16 %2, v[108:111], v[228:231], %2\n\t" \
  "v_add_u32 %4, 64, %4\n\t" \
  "v_add_u32 %5, 64, %5\n\t" \
  "v_mfma_f32_16x16x32_bf16 %3, v[110:113], v[228:231], %3\n\t" \
  "s_sub_u32 %7, %7, 1\n\t" \
  "s_branch LcX%=\n\t" \
  "LtX%=:\n\t" \
  "v_mfma_f32_16x16x32_bf16 %0, v[104:107], v[200:203], %0\n\t" \
  "ds_read_b128 %9, %13 offset:0\n\t" \
  "v_mfma_f32_16x16x32_bf16 %1, v[106:109], v[200:203], %1\n\t" \
  "v_mfma_f32_16x16x32_bf16 %0, v[122:125], v[204:207], %0\n\t" \
  "ds_read_b128 %10, %13 offset:3072\n\t" \
  "v_mfma_f32_16x16x32_bf16 %1, v[124:127], v[204:207], %1\n\t" \
  "v_mfma_f32_16x16x32_bf16 %0, v[140:143], v[208:211], %0\n\t" \
  "v_mfma_f32_16x16x32_bf16 %1, v[142:145], v[208:211], %1\n\t" \
  "v_mfma_f32_16x16x32_bf16 %0, v[158:161], v[212:215], %0\n\t" \
  "v_mfma_f32_16x16x32_bf16 %1, v[160:163], v[212:215], %1\n\t" \
  "v_mfma_f32_16x16x32_bf16 %2, v[104:107], v[212:215], %2\n\t" \
  "v_mfma_f32_16x16x32_bf16 %3, v[106:109], v[212:215], %3\n\t" \
  "s_waitcnt lgkmcnt(0)\n\t" \
  "v_mfma_f32_16x16x32_bf16 %0, v[108:111], v[216:219], %0\n\t" \
  "v_mfma_f32_16x16x32_bf16 %1, v[110:113], v[216:219], %1\n\t" \
  "v_mfma_f32_16x16x32_bf16 %0, v[126:129], v[220:223], %0\n\t" \
  "v_mfma_f32_16x16x32_bf16 %1, v[128:131], v[220:223], %1\n\t" \
  "v_mfma_f32_16x16x32_bf16 %0, v[144:147], v[224:227], %0\n\t" \
  "v_mfma_f32_16x16x32_bf16 %1, v[146:149], v[224:227], %1\n\t" \
  "v_mfma_f32_16x16x32_bf16 %0, v[162:165], v[228:231], %0\n\t" \
  "v_mfma_f32_16x16x32_bf16 %1, v[164:167], v[228:231], %1\n\t" \
  "v_mfma_f32_16x16x32_bf16 %2, v[108:111], v[228:231], %2\n\t" \
  "v_mfma_f32_16x16x32_bf16 %3, v[110:113], v[228:231], %3\n\t" \
  "s_nop 7\n\t" \
  "s_nop 0\n\t"
#define CONV_Y2 \
  "LwY%=:\n\t" \
  "ds_read_b32 v232, %12\n\t" \
  "s_waitcnt lgkmcnt(0)\n\t" \
  "v_readfirstlane_b32 %8, v232\n\t" \
  "s_cmp_lt_u32 %8, 4\n\t" \
  "s_cbranch_scc1 LwY%=\n\t" \
  "ds_read_b64 v[96:97], %4 offset:8\n\t" \
  "ds_read_b128 v[98:101], %4 offset:32\n\t" \
  "ds_read_b128 v[168:171], %6 offset:1024\n\t" \
  "ds_read_b64 v[114:115], %5 offset:8\n\t" \
  "ds_read_b128 v[116:119], %5 offset:32\n\t" \
  "ds_read_b128 v[172:175], %6 offset:2048\n\t" \
  "ds_read_b64 v[132:133], %5 offset:1096\n\t" \
  "ds_read_b128 v[134:137], %5 offset:1120\n\t" \
  "ds_read_b128 v[176:179], %6 offset:3072\n\t" \
  "ds_read_b128 v[102:105], %4 offset:48\n\t" \
  "ds_read_b128 v[184:187], %6 offset:1040\n\t" \
  "ds_read_b128 v[120:123], %5 offset:48\n\t" \
  "ds_read_b128 v[188:191], %6 offset:2064\n\t" \
  "ds_read_b128 v[138:141], %5 offset:1136\n\t" \
  "ds_read_b128 v[192:195], %6 offset:3088\n\t" \
  "LcY%=:\n\t" \
  "s_waitcnt lgkmcnt(6)\n\t" \
  "v_mfma_f32_16x16x32_bf16 %0, v[96:99], v[168:171], %0\n\t" \
  "ds_read_b128 v[106:109], %4 offset:64\n\t" \
  "v_mfma_f32_16x16x32_bf16 %1, v[98:101], v[168:171], %1\n\t" \
  "ds_read_b128 v[200:203], %6 offset:1056\n\t" \
  "v_mfma_f32_16x16x32_bf16 %0, v[114:117], v[172:175], %0\n\t" \
  "ds_read_b128 v[124:127], %5 offset:64\n\t" \
  "v_mfma_f32_16x16x32_bf16 %1, v[116:119], v[172:175], %1\n\t" \
  "ds_read_b128 v[204:207], %6 offset:2080\n\t" \
  "v_mfma_f32_16x16x32_bf16 %2, v[96:99], v[172:175], %2\n\t" \
  "v_mfma_f32_16x16x32_bf16 %3, v[98:101], v[172:175], %3\n\t" \
  "v_mfma_f32_16x16x32_bf16 %0, v[132:135], v[176:179], %0\n\t" \
  "ds_read_b128 v[142:145], %5 offset:1152\n\t" \
  "v_mfma_f32_16x16x32_bf16 %1, v[134:137], v[176:179], %1\n\t" \
  "ds_read_b128 v[208:211], %6 offset:3104\n\t" \
  "v_mfma_f32_16x16x32_bf16 %2, v[114:117], v[176:179], %2\n\t" \
  "v_mfma_f32_16x16x32_bf16 %3, v[116:119], v[176:179], %3\n\t" \
  "s_waitcnt lgkmcnt(6)\n\t" \
  "v_mfma_f32_16x16x32_bf16 %0, v[100:103], v[184:187], %0\n\t" \
  "ds_read_b128 v[110:113], %4 offset:80\n\t" \
  "v_mfma_f32_16x16x32_bf16 %1, v[102:105], v[184:187], %1\n\t" \
  "ds_read_b128 v[216:219], %6 offset:1072\n\t" \
  "v_mfma_f32_16x16x32_bf16 %0, v[118:121], v[188:191], %0\n\t" \
  "ds_read_b128 v[128:131], %5 offset:80\n\t" \
  "v_mfma_f32_16x16x32_bf16 %1, v[120:123], v[188:191], %1\n\t" \
  "ds_read_b128 v[220:223], %6 offset:2096\n\t" \
  "v_mfma_f32_16x16x32_bf16 %2, v[100:103], v[188:191], %2\n\t" \
  "v_mfma_f32_16x16x32_bf16 %3, v[102:105], v[188:191], %3\n\t" \
  "v_mfma_f32_16x16x32_bf16 %0, v[136:139], v[192:195], %0\n\t" \
  "ds_read_b128 v[146:149], %5 offset:1168\n\t" \
  "v_mfma_f32_16x16x32_bf16 %1, v[138:141], v[192:195], %1\n\t" \
  "ds_read_b128 v[224:227], %6 offset:3120\n\t" \
  "v_mfma_f32_16x16x32_bf16 %2, v[118:121], v[192:195], %2\n\t" \
  "v_mfma_f32_16x16x32_bf16 %3, v[120:123], v[192:195], %3\n\t" \
  "s_cmp_eq_u32 %7, 1\n\t" \
  "s_waitcnt lgkmcnt(6)\n\t" \
  "s_cbranch_scc1 LtY%=\n\t" \
  "v_mfma_f32_16x16x32_bf16 %0, v[104:107], v[200:203], %0\n\t" \
  "ds_read_b32 v232, %12 offset:4\n\t" \
  "ds_read_b128 v[98:101], %4 offset:96\n\t" \
  "v_mfma_f32_16x16x32_bf16 %1, v[106:109], v[200:203], %1\n\t" \
  "ds_read_b128 v[168:171], %6 offset:1088\n\t" \
  "v_mfma_f32_16x16x32_bf16 %0, v[122:125], v[204:207], %0\n\t" \
  "ds_read_b128 v[116:119], %5 offset:96\n\t" \
  "v_mfma_f32_16x16x32_bf16 %1, v[124:127], v[204:207], %1\n\t" \
  "ds_read_b128 v[172:175], %6 offset:2112\n\t" \
  "v_mfma_f32_16x16x32_bf16 %2, v[104:107], v[204:207], %2\n\t" \
  "v_mfma_f32_16x16x32_bf16 %3, v[106:109], v[204:207], %3\n\t" \
  "v_mfma_f32_16x16x32_bf16 %0, v[140:143], v[208:211], %0\n\t" \
  "ds_read_b128 v[134:137], %5 offset:1184\n\t" \
  "v_mfma_f32_16x16x32_bf16 %1, v[142:145], v[208:211], %1\n\t" \
  "ds_read_b128 v[176:179], %6 offset:3136\n\t" \
  "v_mfma_f32_16x16x32_bf16 %2, v[122:125], v[208:211], %2\n\t" \
  "v_mfma_f32_16x16x32_bf16 %3, v[124:127], v[208:211], %3\n\t" \
  "s_cmp_lg_u32 %7, %11\n\t" \
  "s_cbranch_scc1 LsY%=\n\t" \
  "s_waitcnt vmcnt(0) lgkmcnt(6)\n\t" \
  "LpY%=:\n\t" \
  "v_readfirstlane_b32 %8, v232\n\t" \
  "s_cmp_ge_u32 %8, 4\n\t" \
  "s_cbranch_scc1 LqY%=\n\t" \
  "ds_read_b32 v232, %12 offset:4\n\t" \
  "s_waitcnt lgkmcnt(0)\n\t" \
  "s_branch LpY%=\n\t" \
  "LqY%=:\n\t" \
  "buffer_store_dwordx2 %15, %14, %16, 0 offen sc1\n\t" \
  "LsY%=:\n\t" \
  "s_waitcnt lgkmcnt(7)\n\t" \
  "v_mfma_f32_16x16x32_bf16 %0, v[108:111], v[216:219], %0\n\t" \
  "ds_read_b128 v[102:105], %4 offset:112\n\t" \
  "v_mov_b64 v[96:97], v[112:113]\n\t" \
  "v_mfma_f32_16x16x32_bf16 %1, v[110:113], v[216:219], %1\n\t" \
  "ds_read_b128 v[184:187], %6 offset:1104\n\t" \
  "v_mfma_f32_16x16x32_bf16 %0, v[126:129], v[220:223], %0\n\t" \
  "ds_read_b128 v[120:123], %5 offset:112\n\t" \
  "v_mov_b64 v[114:115], v[130:131]\n\t" \
  "v_mfma_f32_16x16x32_bf16 %1, v[128:131], v[220:223], %1\n\t" \
  "ds_read_b128 v[188:191], %6 offset:2128\n\t" \
  "v_mfma_f32_16x16x32_bf16 %2, v[108:111], v[220:223], %2\n\t" \
  "v_mfma_f32_16x16x32_bf16 %3, v[110:113], v[220:223], %3\n\t" \
  "v_mfma_f32_16x16x32_bf16 %0, v[144:147], v[224:227], %0\n\t" \
  "ds_read_b128 v[138:141], %5 offset:1200\n\t" \
  "v_mov_b64 v[132:133], v[148:149]\n\t" \
  "v_mfma_f32_16x16x32_bf16 %1, v[146:149], v[224:227], %1\n\t" \
  "ds_read_b128 v[192:195], %6 offset:3152\n\t" \
  "v_add_u32 %6, 64, %6\n\t" \
  "v_mfma_f32_16x16x32_bf16 %2, v[126:129], v[224:227], %2\n\t" \
  "v_add_u32 %4, 64, %4\n\t" \
  "v_add_u32 %5, 64, %5\n\t" \
  "v_mfma_f32_16x16x32_bf16 %3, v[128:131], v[224:227], %3\n\t" \
  "s_sub_u32 %7, %7, 1\n\t" \
  "s_branch LcY%=\n\t" \
  "LtY%=:\n\t" \
  "v_mfma_f32_16x16x32_bf16 %0, v[104:107], v[200:203], %0\n\t" \
  "ds_read_b128 %9, %13 offset:1024\n\t" \
  "v_mfma_f32_16x16x32_bf16 %1, v[106:109], v[200:203], %1\n\t" \
  "v_mfma_f32_16x16x32_bf16 %0, v[122:125], v[204:207], %0\n\t" \
  "ds_read_b128 %10, %13 offset:2048\n\t" \
  "v_mfma_f32_16x16x32_bf16 %1, v[124:127], v[204:207], %1\n\t" \
  "v_mfma_f32_16x16x32_bf16 %2, v[104:107], v[204:207], %2\n\t" \
  "v_mfma_f32_16x16x32_bf16 %3, v[106:109], v[204:207], %3\n\t" \
  "v_mfma_f32_16x16x32_bf16 %0, v[140:143], v[208:211], %0\n\t" \
  "v_mfma_f32_16x16x32_bf16 %1, v[142:145], v[208:211], %1\n\t" \
  "v_mfma_f32_16x16x32_bf16 %2, v[122:125], v[208:211], %2\n\t" \
  "v_mfma_f32_16x16x32_bf16 %3, v[124:127], v[208:211], %3\n\t" \
  "s_waitcnt lgkmcnt(0)\n\t" \
  "v_mfma_f32_16x16x32_bf16 %0, v[108:111], v[216:219], %0\n\t" \
  "v_mfma_f32_16x16x32_bf16 %1, v[110:113], v[216:219], %1\n\t" \
  "v_mfma_f32_16x16x32_bf16 %0, v[126:129], v[220:223], %0\n\t" \
  "v_mfma_f32_16x16x32_bf16 %1, v[128:131], v[220:223], %1\n\t" \
  "v_mfma_f32_16x16x32_bf16 %2, v[108:111], v[220:223], %2\n\t" \
  "v_mfma_f32_16x16x32_bf16 %3, v[110:113], v[220:223], %3\n\t" \
  "v_mfma_f32_16x16x32_bf16 %0, v[144:147], v[224:227], %0\n\t" \
  "v_mfma_f32_16x16x32_bf16 %1, v[146:149], v[224:227], %1\n\t" \
  "v_mfma_f32_16x16x32_bf16 %2, v[126:129], v[224:227], %2\n\t" \
  "v_mfma_f32_16x16x32_bf16 %3, v[128:131], v[224:227], %3\n\t" \
  "s_nop 7\n\t" \
  "s_nop 0\n\t"
#define CONV_X3 \
  "LwX%=:\n\t" \
  "ds_read_b32 v232, %12\n\t" \
  "s_waitcnt lgkmcnt(0)\n\t" \
  "v_readfirstlane_b32 %8, v232\n\t" \
  "s_cmp_lt_u32 %8, 4\n\t" \
  "s_cbranch_scc1 LwX%=\n\t" \
  "ds_read_b64 v[96:97], %4 offset:8\n\t" \
  "ds_read_b128 v[98:101], %4 offset:32\n\t" \
  "ds_read_b128 v[168:171], %6 offset:0\n\t" \
  "ds_read_b64 v[114:115], %5 offset:8\n\t" \
  "ds_read_b128 v[116:119], %5 offset:32\n\t" \
  "ds_read_b128 v[172:175], %6 offset:1024\n\t" \
  "ds_read_b64 v[132:133], %5 offset:1096\n\t" \
  "ds_read_b128 v[134:137], %5 offset:1120\n\t" \
  "ds_read_b128 v[176:179], %6 offset:2048\n\t" \
  "ds_read_b64 v[150:151], %5 offset:2184\n\t" \
  "ds_read_b128 v[152:155], %5 offset:2208\n\t" \
  "ds_read_b128 v[180:183], %6 offset:3072\n\t" \
  "ds_read_b128 v[102:105], %4 offset:48\n\t" \
  "ds_read_b128 v[184:187], %6 offset:16\n\t" \
  "ds_read_b128 v[120:123], %5 offset:48\n\t" \
  "ds_read_b128 v[188:191], %6 offset:1040\n\t" \
  "ds_read_b128 v[138:141], %5 offset:1136\n\t" \
  "ds_read_b128 v[192:195], %6 offset:2064\n\t" \
  "ds_read_b128 v[156:159], %5 offset:2224\n\t" \
  "ds_read_b128 v[196:199], %6 offset:3088\n\t" \
  "LcX%=:\n\t" \
  "s_waitcnt lgkmcnt(8)\n\t" \
  "v_mfma_f32_16x16x32_bf16 %0, v[96:99], v[168:171], %0\n\t" \
  "ds_read_b128 v[106:109], %4 offset:64\n\t" \
  "v_mfma_f32_16x16x32_bf16 %1, v[98:101], v[168:171], %1\n\t" \
  "ds_read_b128 v[200:203], %6 offset:32\n\t" \
  "v_mfma_f32_16x16x32_bf16 %0, v[114:117], v[172:175], %0\n\t" \
  "ds_read_b128 v[124:127], %5 offset:64\n\t" \
  "v_mfma_f32_16x16x32_bf16 %1, v[116:119], v[172:175], %1\n\t" \
  "ds_read_b128 v[204:207], %6 offset:1056\n\t" \
  "v_mfma_f32_16x16x32_bf16 %0, v[132:135], v[176:179], %0\n\t" \
  "ds_read_b128 v[142:145], %5 offset:1152\n\t" \
  "v_mfma_f32_16x16x32_bf16 %1, v[134:137], v[176:179], %1\n\t" \
  "ds_read_b128 v[208:211], %6 offset:2080\n\t" \
  "v_mfma_f32_16x16x32_bf16 %0, v[150:153], v[180:183], %0\n\t" \
  "ds_read_b128 v[160:163], %5 offset:2240\n\t" \
  "v_mfma_f32_16x16x32_bf16 %1, v[152:155], v[180:183], %1\n\t" \
  "ds_read_b128 v[212:215], %6 offset:3104\n\t" \
  "v_mfma_f32_16x16x32_bf16 %2, v[96:99], v[180:183], %2\n\t" \
  "v_mfma_f32_16x16x32_bf16 %3, v[98:101], v[180:183], %3\n\t" \
  "s_waitcnt lgkmcnt(8)\n\t" \
  "v_mfma_f32_16x16x32_bf16 %0, v[100:103], v[184:187], %0\n\t" \
  "ds_read_b128 v[110:113], %4 offset:80\n\t" \
  "v_mfma_f32_16x16x32_bf16 %1, v[102:105], v[184:187], %1\n\t" \
  "ds_read_b128 v[216:219], %6 offset:48\n\t" \
  "v_mfma_f32_16x16x32_bf16 %0, v[118:121], v[188:191], %0\n\t" \
  "ds_read_b128 v[128:131], %5 offset:80\n\t" \
  "v_mfma_f32_16x16x32_bf16 %1, v[120:123], v[188:191], %1\n\t" \
  "ds_read_b128 v[220:223], %6 offset:1072\n\t" \
  "v_mfma_f32_16x16x32_bf16 %0, v[136:139], v[192:195], %0\n\t" \
  "ds_read_b128 v[146:149], %5 offset:1168\n\t" \
  "v_mfma_f32_16x16x32_bf16 %1, v[138:141], v[192:195], %1\n\t" \
  "ds_read_b128 v[224:227], %6 offset:2096\n\t" \
  "v_mfma_f32_16x16x32_bf16 %0, v[154:157], v[196:199], %0\n\t" \
  "ds_read_b128 v[164:167], %5 offset:2256\n\t" \
  "v_mfma_f32_16x16x32_bf16 %1, v[156:159], v[196:199], %1\n\t" \
  "ds_read_b128 v[228:231], %6 offset:3120\n\t" \
  "v_mfma_f32_16x16x32_bf16 %2, v[100:103], v[196:199], %2\n\t" \
  "v_mfma_f32_16x16x32_bf16 %3, v[102:105], v[196:199], %3\n\t" \
  "s_cmp_eq_u32 %7, 1\n\t" \
  "s_waitcnt lgkmcnt(8)\n\t" \
  "s_cbranch_scc1 LtX%=\n\t" \
  "v_mfma_f32_16x16x32_bf16 %0, v[104:107], v[200:203], %0\n\t" \
  "ds_read_b128 v[98:101], %4 offset:96\n\t" \
  "v_mfma_f32_16x16x32_bf16 %1, v[106:109], v[200:203], %1\n\t" \
  "ds_read_b128 v[168:171], %6 offset:64\n\t" \
  "v_mfma_f32_16x16x32_bf16 %0, v[122:125], v[204:207], %0\n\t" \
  "ds_read_b128 v[116:119], %5 offset:96\n\t" \
  "v_mfma_f32_16x16x32_bf16 %1, v[124:127], v[204:207], %1\n\t" \
  "ds_read_b128 v[172:175], %6 offset:1088\n\t" \
  "v_mfma_f32_16x16x32_bf16 %0, v[140:143], v[208:211], %0\n\t" \
  "ds_read_b128 v[134:137], %5 offset:1184\n\t" \
  "v_mfma_f32_16x16x32_bf16 %1, v[142:145], v[208:211], %1\n\t" \
  "ds_read_b128 v[176:179], %6 offset:2112\n\t" \
  "v_mfma_f32_16x16x32_bf16 %0, v[158:161], v[212:215], %0\n\t" \
  "ds_read_b128 v[152:155], %5 offset:2272\n\t" \
  "v_mfma_f32_16x16x32_bf16 %1, v[160:163], v[212:215], %1\n\t" \
  "ds_read_b128 v[180:183], %6 offset:3136\n\t" \
  "v_mfma_f32_16x16x32_bf16 %2, v[104:107], v[212:215], %2\n\t" \
  "v_mfma_f32_16x16x32_bf16 %3, v[106:109], v[212:215], %3\n\t" \
  "s_waitcnt lgkmcnt(8)\n\t" \
  "v_mfma_f32_16x16x32_bf16 %0, v[108:111], v[216:219], %0\n\t" \
  "ds_read_b32 v232, %12 offset:4\n\t" \
  "ds_read_b128 v[102:105], %4 offset:112\n\t" \
  "v_mov_b64 v[96:97], v[112:113]\n\t" \
  "v_mfma_f32_16x16x32_bf16 %1, v[110:113], v[216:219], %1\n\t" \
  "ds_read_b128 v[184:187], %6 offset:80\n\t" \
  "v_mfma_f32_16x16x32_bf16 %0, v[126:129], v[220:223], %0\n\t" \
  "ds_read_b128 v[120:123], %5 offset:112\n\t" \
  "v_mov_b64 v[114:115], v[130:131]\n\t" \
  "v_mfma_f32_16x16x32_bf16 %1, v[128:131], v[220:223], %1\n\t" \
  "ds_read_b128 v[188:191], %6 offset:1104\n\t" \
  "v_mfma_f32_16x16x32_bf16 %0, v[144:147], v[224:227], %0\n\t" \
  "ds_read_b128 v[138:141], %5 offset:1200\n\t" \
  "v_mov_b64 v[132:133], v[148:149]\n\t" \
  "v_mfma_f32_16x16x32_bf16 %1, v[146:149], v[224:227], %1\n\t" \
  "ds_read_b128 v[192:195], %6 offset:2128\n\t" \
  "v_mfma_f32_16x16x32_bf16 %0, v[162:165], v[228:231], %0\n\t" \
  "ds_read_b128 v[156:159], %5 offset:2288\n\t" \
  "v_mov_b64 v[150:151], v[166:167]\n\t" \
  "v_mfma_f32_16x16x32_bf16 %1, v[164:167], v[228:231], %1\n\t" \
  "ds_read_b128 v[196:199], %6 offset:3152\n\t" \
  "v_add_u32 %6, 64, %6\n\t" \
  "v_mfma_f32_16x16x32_bf16 %2, v[108:111], v[228:231], %2\n\t" \
  "v_add_u32 %4, 64, %4\n\t" \
  "v_add_u32 %5, 64, %5\n\t" \
  "v_mfma_f32_16x16x32_bf16 %3, v[110:113], v[228:231], %3\n\t" \
  "s_cmp_lg_u32 %7, %11\n\t" \
  "s_cbranch_scc1 LsX%=\n\t" \
  "s_waitcnt vmcnt(0) lgkmcnt(8)\n\t" \
  "LpX%=:\n\t" \
  "v_readfirstlane_b32 %8, v232\n\t" \
  "s_cmp_ge_u32 %8, 4\n\t" \
  "s_cbranch_scc1 LqX%=\n\t" \
  "ds_read_b32 v232, %12 offset:4\n\t" \
  "s_waitcnt lgkmcnt(0)\n\t" \
  "s_branch LpX%=\n\t" \
  "LqX%=:\n\t" \
  "buffer_store_dwordx2 %15, %14, %16, 0 offen sc1\n\t" \
  "LsX%=:\n\t" \
  "s_sub_u32 %7, %7, 1\n\t" \
  "s_branch LcX%=\n\t" \
  "LtX%=:\n\t" \
  "v_mfma_f32_16x16x32_bf16 %0, v[104:107], v[200:203], %0\n\t" \
  "ds_read_b128 %9, %13 offset:0\n\t" \
  "v_mfma_f32_16x16x32_bf16 %1, v[106:109], v[200:203], %1\n\t" \
  "v_mfma_f32_16x16x32_bf16 %0, v[122:125], v[204:207], %0\n\t" \
  "ds_read_b128 %10, %13 offset:3072\n\t" \
  "v_mfma_f32_16x16x32_bf16 %1, v[124:127], v[204:207], %1\n\t" \
  "v_mfma_f32_16x16x32_bf16 %0, v[140:143], v[208:211], %0\n\t" \
  "v_mfma_f32_16x16x32_bf16 %1, v[142:145], v[208:211], %1\n\t" \
  "v_mfma_f32_16x16x32_bf16 %0, v[158:161], v[212:215], %0\n\t" \
  "v_mfma_f32_16x16x32_bf16 %1, v[160:163], v[212:215], %1\n\t" \
  "v_mfma_f32_16x16x32_bf16 %2, v[104:107], v[212:215], %2\n\t" \
  "v_mfma_f32_16x16x32_bf16 %3, v[106:109], v[212:215], %3\n\t" \
  "s_waitcnt lgkmcnt(0)\n\t" \
  "v_mfma_f32_16x16x32_bf16 %0, v[108:111], v[216:219], %0\n\t" \
  "v_mfma_f32_16x16x32_bf16 %1, v[110:113], v[216:219], %1\n\t" \
  "v_mfma_f32_16x16x32_bf16 %0, v[126:129], v[220:223], %0\n\t" \
  "v_mfma_f32_16x16x32_bf16 %1, v[128:131], v[220:223], %1\n\t" \
  "v_mfma_f32_16x16x32_bf16 %0, v[144:147], v[224:227], %0\n\t" \
  "v_mfma_f32_16x16x32_bf16 %1, v[146:149], v[224:227], %1\n\t" \
  "v_mfma_f32_16x16x32_bf16 %0, v[162:165], v[228:231], %0\n\t" \
  "v_mfma_f32_16x16x32_bf16 %1, v[164:167], v[228:231], %1\n\t" \
  "v_mfma_f32_16x16x32_bf16 %2, v[108:111], v[228:231], %2\n\t" \
  "v_mfma_f32_16x16x32_bf16 %3, v[110:113], v[228:231], %3\n\t" \
  "s_nop 7\n\t" \
  "s_nop 0\n\t"
#define CONV_Y3 \
  "LwY%=:\n\t" \
  "ds_read_b32 v232, %12\n\t" \
  "s_waitcnt lgkmcnt(0)\n\t" \
  "v_readfirstlane_b32 %8, v232\n\t" \
  "s_cmp_lt_u32 %8, 4\n\t" \
  "s_cbranch_scc1 LwY%=\n\t" \
  "ds_read_b64 v[96:97], %4 offset:8\n\t" \
  "ds_read_b128 v[98:101], %4 offset:32\n\t" \
  "ds_read_b128 v[168:171], %6 offset:1024\n\t" \
  "ds_read_b64 v[114:115], %5 offset:8\n\t" \
  "ds_read_b128 v[116:119], %5 offset:32\n\t" \
  "ds_read_b128 v[172:175], %6 offset:2048\n\t" \
  "ds_read_b64 v[132:133], %5 offset:1096\n\t" \
  "ds_read_b128 v[134:137], %5 offset:1120\n\t" \
  "ds_read_b128 v[176:179], %6 offset:3072\n\t" \
  "ds_read_b128 v[102:105], %4 offset:48\n\t" \
  "ds_read_b128 v[184:187], %6 offset:1040\n\t" \
  "ds_read_b128 v[120:123], %5 offset:48\n\t" \
  "ds_read_b128 v[188:191], %6 offset:2064\n\t" \
  "ds_read_b128 v[138:141], %5 offset:1136\n\t" \
  "ds_read_b128 v[192:195], %6 offset:3088\n\t" \
  "LcY%=:\n\t" \
  "s_waitcnt lgkmcnt(6)\n\t" \
  "v_mfma_f32_16x16x32_bf16 %0, v[96:99], v[168:171], %0\n\t" \
  "ds_read_b128 v[106:109], %4 offset:64\n\t" \
  "v_mfma_f32_16x16x32_bf16 %1, v[98:101], v[168:171], %1\n\t" \
  "ds_read_b128 v[200:203], %6 offset:1056\n\t" \
  "v_mfma_f32_16x16x32_bf16 %0, v[114:117], v[172:175], %0\n\t" \
  "ds_read_b128 v[124:127], %5 offset:64\n\t" \
  "v_mfma_f32_16x16x32_bf16 %1, v[116:119], v[172:175], %1\n\t" \
  "ds_read_b128 v[204:207], %6 offset:2080\n\t" \
  "v_mfma_f32_16x16x32_bf16 %2, v[96:99], v[172:175], %2\n\t" \
  "v_mfma_f32_16x16x32_bf16 %3, v[98:101], v[172:175], %3\n\t" \
  "v_mfma_f32_16x16x32_bf16 %0, v[132:135], v[176:179], %0\n\t" \
  "ds_read_b128 v[142:145], %5 offset:1152\n\t" \
  "v_mfma_f32_16x16x32_bf16 %1, v[134:137], v[176:179], %1\n\t" \
  "ds_read_b128 v[208:211], %6 offset:3104\n\t" \
  "v_mfma_f32_16x16x32_bf16 %2, v[114:117], v[176:179], %2\n\t" \
  "v_mfma_f32_16x16x32_bf16 %3, v[116:119], v[176:179], %3\n\t" \
  "s_waitcnt lgkmcnt(6)\n\t" \
  "v_mfma_f32_16x16x32_bf16 %0, v[100:103], v[184:187], %0\n\t" \
  "ds_read_b128 v[110:113], %4 offset:80\n\t" \
  "v_mfma_f32_16x16x32_bf16 %1, v[102:105], v[184:187], %1\n\t" \
  "ds_read_b128 v[216:219], %6 offset:1072\n\t" \
  "v_mfma_f32_16x16x32_bf16 %0, v[118:121], v[188:191], %0\n\t" \
  "ds_read_b128 v[128:131], %5 offset:80\n\t" \
  "v_mfma_f32_16x16x32_bf16 %1, v[120:123], v[188:191], %1\n\t" \
  "ds_read_b128 v[220:223], %6 offset:2096\n\t" \
  "v_mfma_f32_16x16x32_bf16 %2, v[100:103], v[188:191], %2\n\t" \
  "v_mfma_f32_16x16x32_bf16 %3, v[102:105], v[188:191], %3\n\t" \
  "v_mfma_f32_16x16x32_bf16 %0, v[136:139], v[192:195], %0\n\t" \
  "ds_read_b128 v[146:149], %5 offset:1168\n\t" \
  "v_mfma_f32_16x16x32_bf16 %1, v[138:141], v[192:195], %1\n\t" \
  "ds_read_b128 v[224:227], %6 offset:3120\n\t" \
  "v_mfma_f32_16x16x32_bf16 %2, v[118:121], v[192:195], %2\n\t" \
  "v_mfma_f32_16x16x32_bf16 %3, v[120:123], v[192:195], %3\n\t" \
  "s_cmp_eq_u32 %7, 1\n\t" \
  "s_waitcnt lgkmcnt(6)\n\t" \
  "s_cbranch_scc1 LtY%=\n\t" \
  "v_mfma_f32_16x16x32_bf16 %0, v[104:107], v[200:203], %0\n\t" \
  "ds_read_b128 v[98:101], %4 offset:96\n\t" \
  "v_mfma_f32_16x16x32_bf16 %1, v[106:109], v[200:203], %1\n\t" \
  "ds_read_b128 v[168:171], %6 offset:1088\n\t" \
  "v_mfma_f32_16x16x32_bf16 %0, v[122:125], v[204:207], %0\n\t" \
  "ds_read_b128 v[116:119], %5 offset:96\n\t" \
  "v_mfma_f32_16x16x32_bf16 %1, v[124:127], v[204:207], %1\n\t" \
  "ds_read_b128 v[172:175], %6 offset:2112\n\t" \
  "v_mfma_f32_16x16x32_bf16 %2, v[104:107], v[204:207], %2\n\t" \
  "v_mfma_f32_16x16x32_bf16 %3, v[106:109], v[204:207], %3\n\t" \
  "v_mfma_f32_16x16x32_bf16 %0, v[140:143], v[208:211], %0\n\t" \
  "ds_read_b128 v[134:137], %5 offset:1184\n\t" \
  "v_mfma_f32_16x16x32_bf16 %1, v[142:145], v[208:211], %1\n\t" \
  "ds_read_b128 v[176:179], %6 offset:3136\n\t" \
  "v_mfma_f32_16x16x32_bf16 %2, v[122:125], v[208:211], %2\n\t" \
  "v_mfma_f32_16x16x32_bf16 %3, v[124:127], v[208:211], %3\n\t" \
  "s_waitcnt lgkmcnt(6)\n\t" \
  "v_mfma_f32_16x16x32_bf16 %0, v[108:111], v[216:219], %0\n\t" \
  "ds_read_b32 v232, %12 offset:4\n\t" \
  "ds_read_b128 v[102:105], %4 offset:112\n\t" \
  "v_mov_b64 v[96:97], v[112:113]\n\t" \
  "v_mfma_f32_16x16x32_bf16 %1, v[110:113], v[216:219], %1\n\t" \
  "ds_read_b128 v[184:187], %6 offset:1104\n\t" \
  "v_mfma_f32_16x16x32_bf16 %0, v[126:129], v[220:223], %0\n\t" \
  "ds_read_b128 v[120:123], %5 offset:112\n\t" \
  "v_mov_b64 v[114:115], v[130:131]\n\t" \
  "v_mfma_f32_16x16x32_bf16 %1, v[128:131], v[220:223], %1\n\t" \
  "ds_read_b128 v[188:191], %6 offset:2128\n\t" \
  "v_mfma_f32_16x16x32_bf16 %2, v[108:111], v[220:223], %2\n\t" \
  "v_mfma_f32_16x16x32_bf16 %3, v[110:113], v[220:223], %3\n\t" \
  "v_mfma_f32_16x16x32_bf16 %0, v[144:147], v[224:227], %0\n\t" \
  "ds_read_b128 v[138:141], %5 offset:1200\n\t" \
  "v_mov_b64 v[132:133], v[148:149]\n\t" \
  "v_mfma_f32_16x16x32_bf16 %1, v[146:149], v[224:227], %1\n\t" \
  "ds_read_b128 v[192:195], %6 offset:3152\n\t" \
  "v_add_u32 %6, 64, %6\n\t" \
  "v_mfma_f32_16x16x32_bf16 %2, v[126:129], v[224:227], %2\n\t" \
  "v_add_u32 %4, 64, %4\n\t" \
  "v_add_u32 %5, 64, %5\n\t" \
  "v_mfma_f32_16x16x32_bf16 %3, v[128:131], v[224:227], %3\n\t" \
  "s_cmp_lg_u32 %7, %11\n\t" \
  "s_cbranch_scc1 LsY%=\n\t" \
  "s_waitcnt vmcnt(0) lgkmcnt(6)\n\t" \
  "LpY%=:\n\t" \
  "v_readfirstlane_b32 %8, v232\n\t" \
  "s_cmp_ge_u32 %8, 4\n\t" \
  "s_cbranch_scc1 LqY%=\n\t" \
  "ds_read_b32 v232, %12 offset:4\n\t" \
  "s_waitcnt lgkmcnt(0)\n\t" \
  "s_branch LpY%=\n\t" \
  "LqY%=:\n\t" \
  "buffer_store_dwordx2 %15, %14, %16, 0 offen sc1\n\t" \
  "LsY%=:\n\t" \
  "s_sub_u32 %7, %7, 1\n\t" \
  "s_branch LcY%=\n\t" \
  "LtY%=:\n\t" \
  "v_mfma_f32_16x16x32_bf16 %0, v[104:107], v[200:203], %0\n\t" \
  "ds_read_b128 %9, %13 offset:1024\n\t" \
  "v_mfma_f32_16x16x32_bf16 %1, v[106:109], v[200:203], %1\n\t" \
  "v_mfma_f32_16x16x32_bf16 %0, v[122:125], v[204:207], %0\n\t" \
  "ds_read_b128 %10, %13 offset:2048\n\t" \
  "v_mfma_f32_16x16x32_bf16 %1, v[124:127], v[204:207], %1\n\t" \
  "v_mfma_f32_16x16x32_bf16 %2, v[104:107], v[204:207], %2\n\t" \
  "v_mfma_f32_16x16x32_bf16 %3, v[106:109], v[204:207], %3\n\t" \
  "v_mfma_f32_16x16x32_bf16 %0, v[140:143], v[208:211], %0\n\t" \
  "v_mfma_f32_16x16x32_bf16 %1, v[142:145], v[208:211], %1\n\t" \
  "v_mfma_f32_16x16x32_bf16 %2, v[122:125], v[208:211], %2\n\t" \
  "v_mfma_f32_16x16x32_bf16 %3, v[124:127], v[208:211], %3\n\t" \
  "s_waitcnt lgkmcnt(0)\n\t" \
  "v_mfma_f32_16x16x32_bf16 %0, v[108:111], v[216:219], %0\n\t" \
  "v_mfma_f32_16x16x32_bf16 %1, v[110:113], v[216:219], %1\n\t" \
  "v_mfma_f32_16x16x32_bf16 %0, v[126:129], v[220:223], %0\n\t" \
  "v_mfma_f32_16x16x32_bf16 %1, v[128:131], v[220:223], %1\n\t" \
  "v_mfma_f32_16x16x32_bf16 %2, v[108:111], v[220:223], %2\n\t" \
  "v_mfma_f32_16x16x32_bf16 %3, v[110:113], v[220:223], %3\n\t" \
  "v_mfma_f32_16x16x32_bf16 %0, v[144:147], v[224:227], %0\n\t" \
  "v_mfma_f32_16x16x32_bf16 %1, v[146:149], v[224:227], %1\n\t" \
  "v_mfma_f32_16x16x32_bf16 %2, v[126:129], v[224:227], %2\n\t" \
  "v_mfma_f32_16x16x32_bf16 %3, v[128:131], v[224:227], %3\n\t" \
  "s_nop 7\n\t" \
  "s_nop 0\n\t"
#define CONV_CLOB "v96","v97","v98","v99","v100","v101","v102","v103","v104","v105","v106","v107","v108","v109","v110","v111","v112","v113","v114","v115","v116","v117","v118","v119","v120","v121","v122","v123","v124","v125","v126","v127","v128","v129","v130","v131","v132","v133","v134","v135","v136","v137","v138","v139","v140","v141","v142","v143","v144","v145","v146","v147","v148","v149","v150","v151","v152","v153","v154","v155","v156","v157","v158","v159","v160","v161","v162","v163","v164","v165","v166","v167","v168","v169","v170","v171","v172","v173","v174","v175","v176","v177","v178","v179","v180","v181","v182","v183","v184","v185","v186","v187","v188","v189","v190","v191","v192","v193","v194","v195","v196","v197","v198","v199","v200","v201","v202","v203","v204","v205","v206","v207","v208","v209","v210","v211","v212","v213","v214","v215","v216","v217","v218","v219","v220","v221","v222","v223","v224","v225","v226","v227","v228","v229","v230","v231","v232"
#define RLX_AGENT __ATOMIC_RELAXED, __HIP_MEMORY_SCOPE_AGENT

#define P_OAUX 19
#define P_SLB 4
#define P_XP 1
#define P_FLIP 0
#define P_PS 1
#define P_PA 35
#define P_PB 36
#define P_TPI 42
#define P_SPN 2
#define P_SPY 1
#define P_LZ 3
#define P_J1 5
#define P_J2 9
#define P_J3 13
#define P_J4 17
#define P_J5 20
#define P_JB 4
#define P_HA 16
#define P_HS 16
#define P_DA ""
#define P_EE 0
#define P_AP 0
#define P_DF 0
#define P_C3 1
#define P_BL 8
#define P_FP 35
#define P_SL0 22
#define P_EU 16
#define P_PRE 2
#define P_SP 8
#define P_G0 6
#define P_I0 2
#define P_I1 2
#define P_I2 2
#define P_GX 0
#define P_EC 8
#define P_G1 (P_G0 + P_I0 + P_I1 + P_PRE + P_I2 + P_GX)
#define P_WIN 6
#define P_FS 128
#define P_SLA 4
#define P_N0 14
#define P_LEAD 9
#define P_LEAD1 P_LEAD
#define P_LATE 4
#define P_PUB 2
#define P_EPF 8
#define P_UP 4096
#define P_US 2
#define P_UD 4
#define P_NTRY 4
#define P_S1 2
#define P_AG 10
#define P_XW 55
#define P_XH 55
#define P_XG 44
#define P_XF 57
#define P_SU 31
#define P_SG 22
#define P_TS 8
#define P_WA 12
#define P_WR 13
#define P_EI 22
#define P_UA 16
#define P_YA 17
#define P_TY 18000
#define P_WP 0
#define P_HC 31
#define P_LS 1
#define P_BP P_WP
#define P_WQ 99
#define P_W0 9
#define P_R 4
#define P_EB 2
#define P_YI 25
#define P_YN 4
#define P_SPIN 200000

constexpr int S_ = 2048, D_ = 512, F_ = 1376;
constexpr u32 WS_ECOL = 0, WS_EROW = 1024, WS_FU = 4096, WS_FH = WS_FU + 256 * P_FS, WS_ST = WS_FH + 512 * P_FS;
constexpr u32 UP = P_UP, WS_U = 262144, WS_H = WS_U + 512 * UP, WS_END = WS_H + 64 * 86 * 1024;
static_assert(WS_ST + 32768 <= WS_U, "ctrl");
constexpr u32 XS = 16384, L_W = 4 * XS, WSL = 12288, L_HC = L_W + 4 * WSL, HCS = 4416, L_ZB = L_HC + 8 * HCS + 48, L_CNT = L_ZB + 288, L_UL = L_ZB + 304, ULS = 4352;
constexpr u32 L_CS = L_UL + 2 * ULS, L_MS = L_CS + 96 * 16, L_GA = L_MS + 128 * 16, L_END = L_GA + 128 * 8;
static_assert(L_END <= 163840 && (L_UL & 31) == 0 && (L_CS & 15) == 0, "lds");
constexpr int imin(int a, int b) { return a < b ? a : b; }
constexpr int ncum(int c) { return c == 0 ? P_N0 : c == 1 ? 14 + P_LEAD1 : c < 8 ? imin(7 * (c + 1) + P_LEAD, 56 + P_LATE) : c == 8 ? 56 + P_LATE : 56 + P_G0; }
constexpr int gend(int s) {
  const int c = (s + 2) >> 2, st0 = c == 0 ? 0 : 4 * c - 2, nst = (c == 0 || c == 8) ? 2 : 4;
  return ncum(c) + ((ncum(c + 1) - ncum(c)) * (s - st0 + 1) + nst - 1) / nst;
}
constexpr int nex(int g) {
  return 3 * (g == P_XH) + (g == P_XW) + (g == P_XG) + (g == P_XF);
}
constexpr int nu(int s, int g) {
  return 2 * (P_SU < s && g < gend(imin(P_SU, 31))) + (P_S1 + 1 < s && g < gend(P_S1 + 1)) + (P_AG < s && g < gend(P_AG));
}
constexpr int nops(int a, int b) { int n = 0; for (int g = a; g < b; ++g) n += 1 + nex(g); return n; }
constexpr int younger(int g, int e) { return nops(g, e) - 1; }
constexpr u32 pofs(int g) { return g % 7 < 4 ? ((g / 7) & 3) * XS + (g % 7) * 1024 : ((g / 7) & 3) * WSL + (g % 7 - 4) * 1024; }
constexpr int gu(int i) { return i < 44 - 2 * P_TS ? i >> 1 : i < 44 - P_TS ? i - 22 + P_TS : i - 22; }
constexpr int gx(int i) { return i < 44 - 2 * P_TS ? i & 1 : i >= 44 - P_TS; }
constexpr int gl(int i) { return i + (i < P_R ? P_W0 : imin(P_WA, P_W0 + i - P_R + 1)); }
constexpr u32 L_K = L_HC, L_V = L_K + 68 * 128, L_XR = L_V + 64 * 128, L_UT = L_XR + 8192;
static_assert(L_UT + 8192 <= L_ZB, "lds");

#define SB() __builtin_amdgcn_sched_barrier(0)
#define ST(k) do {} while (0)
#define STI(k) do {} while (0)
#define STX(k) do {} while (0)
#define STL(k) do {} while (0)

__device__ __forceinline__ void dma16(u32 lds, u32 voff, u32x4 srd, u32 soff) {
  asm volatile("s_mov_b32 m0, %0\n\ts_nop 0\n\tbuffer_load_dwordx4 %1, %2, %3 offen lds" ::"s"(lds), "v"(voff), "s"(srd), "s"(soff) : "memory");
}
__device__ __forceinline__ void dmai(u32 lds, u32 LO, u32 voff, u32x4 srd, u32 soff, u32 SO) {
  u32 t;
  asm volatile("s_add_u32 m0, %1, %2\n\ts_add_u32 %0, %5, %6\n\tbuffer_load_dwordx4 %3, %4, %0 offen" P_DA " lds"
               : "=&s"(t) : "s"(lds), "i"(LO), "v"(voff), "s"(srd), "s"(soff), "i"(SO) : "scc", "memory");
}
__device__ __forceinline__ void dma16c(u32 lds, u32 voff, u32x4 srd, u32 soff) {
  asm volatile("s_mov_b32 m0, %0\n\ts_nop 0\n\tbuffer_load_dwordx4 %1, %2, %3 offen sc1 lds" ::"s"(lds), "v"(voff), "s"(srd), "s"(soff) : "memory");
}
__device__ __forceinline__ void ld16(u32x4& d, u32 voff, u32x4 srd, u32 soff) {
  asm volatile("buffer_load_dwordx4 %0, %1, %2, %3 offen" : "+v"(d) : "v"(voff), "s"(srd), "s"(soff) : "memory");
}
__device__ __forceinline__ void ld16c(u32x4& d, u32 voff, u32x4 srd) {
  asm volatile("buffer_load_dwordx4 %0, %1, %2, 0 offen sc1" : "+v"(d) : "v"(voff), "s"(srd) : "memory");
}
__device__ __forceinline__ void ld8c(u32x2& d, u32 voff, u32x4 srd) {
  asm volatile("buffer_load_dwordx2 %0, %1, %2, 0 offen sc1" : "+v"(d) : "v"(voff), "s"(srd) : "memory");
}
template <int N> __device__ __forceinline__ void wait_vm() { asm volatile("s_waitcnt vmcnt(%0)" ::"i"(N) : "memory"); }
__device__ __forceinline__ void wg_barrier() { asm volatile("s_waitcnt lgkmcnt(0)\n\ts_barrier" ::: "memory"); }
__device__ __forceinline__ u32x4 mk_srd(const void* p, u32 n) {
  u64 a = (u64)p; u32x4 r; r.x = (u32)a; r.y = (u32)(a >> 32); r.z = n; r.w = 0x00020000u; return r;
}
__device__ __forceinline__ __amdgpu_buffer_rsrc_t mk_rs(const void* p, u32 n) {
  return __builtin_amdgcn_make_buffer_rsrc((void*)p, 0, n, 0x00020000);
}
template <class T> __device__ __forceinline__ T lds_ld(u32 a) { return *(const AS3 T*)(uintptr_t)a; }
template <class T> __device__ __forceinline__ void lds_st(u32 a, T v) { *(AS3 T*)(uintptr_t)a = v; }
__device__ __forceinline__ u32x2 lds_tr(u32 a) {
  return __builtin_bit_cast(u32x2, __builtin_amdgcn_ds_read_tr16_b64_v4i16((AS3 s16x4*)(uintptr_t)a));
}
__device__ __forceinline__ u32x4 cat(u32x2 a, u32x2 b) { u32x4 r; r.x = a.x; r.y = a.y; r.z = b.x; r.w = b.y; return r; }
__device__ __forceinline__ f32x16 mfma32(u32x4 a, u32x4 b, f32x16 c) {
  return __builtin_amdgcn_mfma_f32_32x32x16_bf16(__builtin_bit_cast(bf8, a), __builtin_bit_cast(bf8, b), c, 0, 0, 0);
}
__device__ __forceinline__ f32x4 mfma16(u32x4 a, u32x4 b, f32x4 c) {
  return __builtin_amdgcn_mfma_f32_16x16x32_bf16(__builtin_bit_cast(bf8, a), __builtin_bit_cast(bf8, b), c, 0, 0, 0);
}
__device__ __forceinline__ float dot2(u32 a, u32 b, float c) {
  return __builtin_amdgcn_fdot2_f32_bf16(__builtin_bit_cast(bf2, a), __builtin_bit_cast(bf2, b), c, false);
}
__device__ __forceinline__ u32 pk(float lo, float hi) {
  f32x2 v; v.x = lo; v.y = hi; return __builtin_bit_cast(u32, __builtin_convertvector(v, bf2));
}
__device__ __forceinline__ float blo(u32 v) { return __builtin_bit_cast(float, v << 16); }
__device__ __forceinline__ float bhi(u32 v) { return __builtin_bit_cast(float, v & 0xffff0000u); }
__device__ __forceinline__ u32 f2u(float v) { return __builtin_bit_cast(u32, v); }
__device__ __forceinline__ float u2f(u32 v) { return __builtin_bit_cast(float, v); }
__device__ __forceinline__ float swap32sum(float a, float b) {
  auto r = __builtin_amdgcn_permlane32_swap(f2u(a), f2u(b), false, false);
  const u32 r0 = r[0], r1 = r[1];
  return u2f(r0) + u2f(r1);
}
__device__ __forceinline__ float swap16sum(float a, float b) {
  auto r = __builtin_amdgcn_permlane16_swap(f2u(a), f2u(b), false, false);
  const u32 r0 = r[0], r1 = r[1];
  return u2f(r0) + u2f(r1);
}
template <int C> __device__ __forceinline__ float dppadd(float v) {
  return v + u2f(__builtin_amdgcn_update_dpp(0u, f2u(v), C, 0xf, 0xf, true));
}
__device__ __forceinline__ u64 next_tag(u64 e) { return e * 6364136223846793005ull + 1442695040888963407ull; }
__device__ __forceinline__ bool same8(u32x4 a, u32x4 o) { return (a.x == o.x && a.y == o.y) || (a.z == o.z && a.w == o.w); }
constexpr u32 ONES = 0x3f803f80u;

__device__ __forceinline__ u32x4 aug_a(f32x4 c, float b1) {
  const float cs = (c.x + c.y) + (c.z + c.w);
  u32x4 a; a.x = pk(cs, b1); a.y = a.x; a.z = pk(cs - blo(a.x), 0.f); a.w = 0u;
  return a;
}
struct Gk { float k1, k2, ir; };
__device__ __forceinline__ bool tagged(u32x4 g, u64 tag) { return __all(g.y == (u32)tag && g.w == (u32)(tag >> 32)); }
__device__ __forceinline__ Gk gelu_k(float r, float ir) {
  Gk g; g.k1 = r * -2.302208198f; g.k2 = r * r * r * -0.1029432397f; g.ir = ir; return g;
}
__device__ __forceinline__ void gelu_half(const f32x16& acc, int s, Gk g, __amdgpu_buffer_rsrc_t rws, u32 voff) {
  u32x4 v;
#pragma unroll
  for (int d = 0; d < 4; ++d) {
    f32x2 a; a.x = acc[8 * s + 2 * d]; a.y = acc[8 * s + 2 * d + 1];
    const f32x2 z = a * __builtin_elementwise_fma(a * a, (f32x2)(g.k2), (f32x2)(g.k1));
    f32x2 e; e.x = __builtin_amdgcn_exp2f(z.x); e.y = __builtin_amdgcn_exp2f(z.y);
    e = __builtin_elementwise_fma(e, (f32x2)(g.ir), (f32x2)(g.ir));
    f32x2 q; q.x = __builtin_amdgcn_rcpf(e.x); q.y = __builtin_amdgcn_rcpf(e.y);
    q *= a;
    v[d] = pk(q.x, q.y);
  }
  __builtin_amdgcn_raw_buffer_store_b128(v, rws, voff + s * 1024, 0, P_HS);
  SB();
}
#define CAT_(a, b) a##b
#define CAT(a, b) CAT_(a, b)
#define PUBS 2
__device__ __forceinline__ void conv_run(bool X, u32 a0, u32 a1, u32 bb, f32x4 (&acc)[2][2], u32x4 (&u)[2], u32 ua, int pub, u32 fo, u64 tag, u32x4 srd) {
  u32 tmp;
  int n = 4;
  if (X) asm volatile(CAT(CONV_X, PUBS) : "+v"(acc[0][0]), "+v"(acc[0][1]), "+v"(acc[1][0]), "+v"(acc[1][1]), "+v"(a0), "+v"(a1), "+v"(bb), "+&s"(n), "=&s"(tmp),
                      "=&v"(u[0]), "=&v"(u[1]) : "s"(pub), "v"(L_CNT + 4), "v"(ua), "v"(fo), "v"(tag), "s"(srd) : CONV_CLOB, "scc", "memory");
  else asm volatile(CAT(CONV_Y, PUBS) : "+v"(acc[0][0]), "+v"(acc[0][1]), "+v"(acc[1][0]), "+v"(acc[1][1]), "+v"(a0), "+v"(a1), "+v"(bb), "+&s"(n), "=&s"(tmp),
                    "=&v"(u[0]), "=&v"(u[1]) : "s"(pub), "v"(L_CNT + 4), "v"(ua), "v"(fo), "v"(tag), "s"(srd) : CONV_CLOB, "scc", "memory");
}
__device__ __forceinline__ u32x2 split(u64 v) { u32x2 r; r.x = (u32)v; r.y = (u32)(v >> 32); return r; }
__device__ __forceinline__ void lds_inc(u32 cnt, int lane) {
  asm volatile("s_waitcnt lgkmcnt(0)" ::: "memory");
  if (lane == 0) __hip_atomic_fetch_add((AS3 u32*)(uintptr_t)cnt, 1u, __ATOMIC_RELAXED, __HIP_MEMORY_SCOPE_WORKGROUP);
}
template <int M> __device__ __forceinline__ void lds_post(u32 cnt, u32 one) {
  asm volatile("s_mov_b64 exec, %2\n\tds_add_u32 %0, %1\n\ts_mov_b64 exec, -1" ::"v"(cnt), "v"(one), "i"(M) : "memory");
}
__device__ __forceinline__ void lds_wait(u32 cnt, u32 n) {
#pragma unroll 1
  for (int it = 0; it < P_SPIN * 8; ++it)
    if ((u32)__builtin_amdgcn_readfirstlane(__hip_atomic_load((AS3 u32*)(uintptr_t)cnt, __ATOMIC_RELAXED, __HIP_MEMORY_SCOPE_WORKGROUP)) >= n) break;
  asm volatile("" ::: "memory");
}
template <int SL> __device__ __forceinline__ void lds_spin(u32 cnt, u32 n) {
  u32 t;
  asm volatile("ds_read_b32 %0, %1\n\ts_waitcnt lgkmcnt(0)\n\tv_cmp_le_u32 vcc, %2, %0\n\ts_cbranch_vccnz Lsd%=\n"
               "Lsp%=:\n\ts_cmp_lg_u32 %2, %3\n\ts_cbranch_scc1 Lsn%=\n\ts_sleep 1\nLsn%=:\n\tds_read_b32 %0, %1\n\ts_waitcnt lgkmcnt(0)\n\tv_cmp_le_u32 vcc, %2, %0\n\ts_cbranch_vccz Lsp%=\nLsd%=:"
               : "=&v"(t) : "v"(cnt), "s"(n), "i"(SL) : "vcc", "scc", "memory");
}
template <int V> struct IC { static constexpr int value = V; };
template <int N, int K = 0, class Fn> __device__ __forceinline__ void static_for(Fn&& f) {
  if constexpr (K < N) { f(IC<K>{}); static_for<N, K + 1>(f); }
}

struct Srd { const char* p; u32 n, f; };
struct KArgs { Srd k, v, q, w1, ws; const char *h, *sk, *b1, *D, *w2, *b2, *out; };
__device__ __forceinline__ const char* kptr(u32x16 v, int n) { return (const char*)(((u64)v[2 * n + 1] << 32) | v[2 * n]); }
template <int N> __device__ __forceinline__ u32x4 ksrd(u32x16 v) { return __builtin_shufflevector(v, v, 4 * N, 4 * N + 1, 4 * N + 2, 4 * N + 3); }

__global__ __launch_bounds__(512, 2) __attribute__((aligned(4096))) void fused_kernel(KArgs args) {
  ST(0);
  u32x16 ka0, ka1; char* pout;
  asm volatile("s_load_dwordx16 %0, %3, 0x0\n\ts_load_dwordx16 %1, %3, 0x40\n\ts_load_dwordx2 %2, %3, 0x80"
               : "=&s"(ka0), "=&s"(ka1), "=&s"(pout) : "s"(__builtin_amdgcn_kernarg_segment_ptr()));
  SB();
  int tid = threadIdx.x, b = blockIdx.x ^ P_FLIP;
  u32 epf;
  {
    u64 pc0; u32 len;
    asm volatile("s_getpc_b64 %0\nLcode_begin:\n\ts_mov_b32 %1, Lcode_end-Lcode_begin" : "=s"(pc0), "=s"(len));
    asm volatile("buffer_load_dword %0, %1, %2, 0 offen" : "={v255}"(epf) : "v"(((tid << 6) & ~511) | ((b & 24) << 4)), "s"(mk_srd((const void*)pc0, b < 8 * P_EPF ? len : 0u)) : "memory");
    SB();
  }
  int wv = __builtin_amdgcn_readfirstlane(tid) >> 6;
  const int rr = b >> 3, I = 2 * (b & 6) + (rr >> 3), J = 8 * (b & 1) + (rr & 7);
  const int tj = rr & 7, ti = 2 * I + (b & 1);
  const int unit0 = 3 * J - (J >> 2) - ((J + 1) >> 4);
  const bool light = (0xc888 >> J) & 1;
  u32 vx[2], vw[3], vW2; u32x2 sk;
  auto mkvw = [&](int q) {
    const u32 el = q * 64 + (tid & 63), row = (el * 171) >> 11;
    if (P_LZ & 2) vw[q] = (row * (F_ * 2) + (el - row * 12) * 16) | (((el - row * 12) << 28) & (light ? 0x80000000u : 0u));
    else vw[q] = row * (F_ * 2) + min(el - row * 12, light ? 7u : 11u) * 16;
  };
  vx[0] = ((tid << 7) & 0x1c00u) | (((tid ^ ((tid >> 4) & 3)) & 7) << 4); vx[1] = vx[0] ^ 64;
  vW2 = (((tid << 7) & 0x1c00u) | ((tid & 7) << 4)) ^ ((tid & 16) << 2);
  mkvw(0); mkvw(1);
  {
    u32 v1 = (vW2 ^ ((wv & 2) << 5)) + (64 * ti - 4 + wv) * 1024;
    u32 sk_ = (64 * ti + 8 * wv) * 1024 + tj * 128, sx_ = (128 * I + 16 * wv) * 1024, sw_ = 8 * wv * (F_ * 2) + unit0 * 64;
    u32 d0 = wv * 128, d1 = wv * 1024, d2 = wv * 2048, d3 = wv * 1536, st_ = tj * 128, hx = wv < 4 ? 0xffu : 0u;
    u32 v2 = (tid & 31) * 8 + tj * 512 + (wv & 2) * 128;
    asm volatile("" : "+v"(vx[0]), "+v"(vx[1]), "+v"(vw[0]), "+v"(vw[1]), "+v"(vW2), "+v"(v1), "+v"(v2));
    asm volatile("" : "+s"(sk_), "+s"(sx_), "+s"(sw_), "+s"(d0), "+s"(d1), "+s"(d2), "+s"(d3), "+s"(st_), "+s"(hx));
    asm volatile("s_waitcnt lgkmcnt(0)" : "+s"(ka0), "+s"(ka1), "+s"(pout));
    ST(28);
    dmai(d1, L_K + 512, vW2, ksrd<0>(ka0), sk_, 0);
    dmai(d1, L_V, vW2, ksrd<1>(ka0), sk_, 0);
    asm volatile("global_load_dwordx2 %0, %1, %2" : "={v[252:253]}"(sk) : "v"(v2), "s"(kptr(ka1, 3)) : "memory");
    asm volatile("s_mov_b32 exec_hi, 0\n\ts_mov_b32 exec_lo, %0" ::"s"(hx));
    dmai(d0, L_K, v1, ksrd<0>(ka0), st_, 0);
    asm volatile("s_mov_b64 exec, -1");
    dmai(d2, 0, vx[0], ksrd<2>(ka0), sx_, 0);
    dmai(d2, 1024, vx[1], ksrd<2>(ka0), sx_, 8192);
    dmai(d3, L_W, vw[0], ksrd<3>(ka0), sw_, 0);
    asm volatile("s_mov_b32 exec_hi, 0");
    dmai(d3, L_W + 1024, vw[1], ksrd<3>(ka0), sw_, 0);
    asm volatile("s_mov_b32 exec_hi, -1");
  }
  SB();
  ST(29);
  const u32x4 srdX = ksrd<2>(ka0), srdW1 = ksrd<3>(ka0), srdS = ksrd<0>(ka1);
  char* const ws = (char*)kptr(ka1, 0);
  int w, ch0;
  u32 dX, dW, hbase;
  auto indices = [&]() {
    asm volatile("" : "+s"(wv));
    asm volatile("" : "+v"(tid));
    w = wv & 3;
    ch0 = 64 * tj + 2 * ti;
    dX = w * 4096; dW = L_W + w * 3072;
    hbase = WS_H + ((4 * I + w) * 86 + 2 * unit0) * 1024;
  };
  u64 tagR, tagC;
  u32 xa[4], wk[4]; u32x4 sel;
  auto setup = [&](int lane, int role) {
    const int hh = lane >> 5, row = 32 * w + (lane & 31);
    const u32 base = row * 128 + ((hh ^ ((row >> 1) & 7)) << 4);
    const u32 wa = L_W + role * 128 + (8 * hh + ((lane >> 2) & 3)) * 192 + ((lane >> 4) & 1) * 32 + (lane & 3) * 8;
#pragma unroll
    for (int ks = 0; ks < 4; ++ks) { xa[ks] = base ^ (((ks + w) & 3) * 32); wk[ks] = wa + ((ks + w) & 3) * 3072; }
    if ((P_LZ & 1) && role == 1) {
      const u32 off = light ? 0x40000u : 0u;
#pragma unroll
      for (int ks = 0; ks < 4; ++ks) { xa[ks] |= off; wk[ks] |= off; }
    }
    sel.x = (lane & 15) == ((lane >> 2) & 4) ? ONES : 0u; sel.y = sel.x; sel.z = sel.x; sel.w = sel.x;
    asm volatile("" : "+v"(xa[0]), "+v"(xa[1]), "+v"(xa[2]), "+v"(xa[3]), "+v"(wk[0]), "+v"(wk[1]), "+v"(wk[2]), "+v"(wk[3]), "+v"(sel));
  };
  float xf[8], xm[8], b2f[8];
  u32 exo, uta, cwa, cwn;
  u32 sx0, sw0;
  auto g1dma = [&](auto G) {
    constexpr int g = G.value, ch = g / 7, q = g % 7;
    if constexpr (q < 4) dmai(dX, pofs(g), vx[q & 1], srdX, sx0, q * 8192 + ch * 128);
    else dmai(dW, pofs(g), vw[q - 4], srdW1, sw0, ch * (64 * F_ * 2));
  };
  u32x4 y0, y1, uo[2]; bool pend = false; u32 ep0, ep1, epy, ya;
  auto yload = [&]() {
    const u32 so = WS_U + 64 * tj * UP + (31 - ti) * 128, yvo = (tid >> 3) * (2 * UP) + (tid & 7) * 16;
    y0 = __builtin_amdgcn_raw_buffer_load_b128(mk_rs(ws, WS_END), yvo, so, 16);
    y1 = __builtin_amdgcn_raw_buffer_load_b128(mk_rs(ws, WS_END), yvo, so + UP, 16);
  };
  auto ytrans = [&]() {
    ST(13);
#pragma unroll
    for (int d = 0; d < 4; ++d) {
      lds_st<u32>(ya + d * 256, __builtin_amdgcn_perm(y1[d], y0[d], 0x05040100u));
      lds_st<u32>(ya + d * 256 + 128, __builtin_amdgcn_perm(y1[d], y0[d], 0x07060302u));
    }
    lds_post<1>(L_CNT + 12, 1u);
  };
  auto epoch = [&]() {
    if (tid == 0) {
      if (ti == 0) __hip_atomic_store((AS1 u64*)(ws + WS_ECOL + tj * 128), tagC, RLX_AGENT);
      if (J == 0) __hip_atomic_store((AS1 u64*)(ws + WS_EROW + I * 128), tagR, RLX_AGENT);
    }
  };
  auto pre_ms = [&]() { return lds_ld<f32x2>(L_MS + (64 * (b & 1) + (tid >> 3)) * 16); };
  auto pre_epi = [&](int d, u32x4 ex, u32x4 eb, f32x2 ms) {
    xf[d] = d & 1 ? bhi(ex[d >> 1]) : blo(ex[d >> 1]); b2f[d] = d & 1 ? bhi(eb[d >> 1]) : blo(eb[d >> 1]);
    xm[d] = (xf[d] - ms.x) * ms.y;
    asm volatile("" : "+v"(xm[d]), "+v"(xf[d]), "+v"(b2f[d]));
  };
  auto pre_adr = [&]() {
    const int t64 = tid >> 3, cg = tid & 7, t = t64 & 31, f = ((t >> 1) & 3) | ((t & 1) << 2);
    const u32 pa = (2 * (cg >> 2) + (t64 >> 5)) * XS + t * 128;
    ep0 = pa + (((2 * (cg & 3)) ^ f) << 4); ep1 = ep0 ^ 16;
    epy = L_UL + t64 * 128 + ((cg ^ (t64 >> 3)) << 4);
    asm volatile("" : "+v"(ep0), "+v"(ep1), "+v"(epy));
  };

  if (__builtin_expect(wv < 4, 1)) {
    indices();
    const int lane = tid & 63, hh = lane >> 5;
    u32 pf1, b1v;
    const char *wsl = ws, *pDl = kptr(ka1, 5);
    asm volatile("" : "+s"(wsl), "+s"(pDl)::"memory");
    u64 eR = *(const AS4 u64*)(wsl + WS_EROW + I * 128), eC = *(const AS4 u64*)(wsl + WS_ECOL + tj * 128);
    u32 dvv = *(const AS4 u32*)(pDl + ch0 * 2);
    u64 t0 = 0;
    if (P_TY && !P_YI) t0 = __builtin_amdgcn_s_memtime();
    u32 b2t = *(const AS4 u32*)(kptr(ka1, 7) + tj * 128 + (w & 1) * 64);
    SB();
    const __amdgpu_buffer_rsrc_t rws = mk_rs(ws, WS_END);
    {
      const u32 zb = w == 0 ? L_ZB : L_UL - ULS + 4096 + w * ULS, zn = w == 0 ? 19 : w == 3 ? 0 : 16;
      if ((u32)lane < zn) lds_st<u32x4>(zb + lane * 16, u32x4{0, 0, 0, 0});
    }
    setup(lane, 0);
    exo = (64 * ti + (tid >> 3)) * 1024 + tj * 128 + (tid & 7) * 16;
    f32x16 acc0, acc1;
    f32x4 cs0 = {0.f, 0.f, 0.f, 0.f};
    u32x4 sel1; sel1.x = (lane & 15) == ((lane >> 2) & 4) + 8 ? ONES : 0u; sel1.y = sel1.x; sel1.z = sel1.x; sel1.w = sel1.x;
    u32 gka = L_MS + 8 + (32 * w + (lane & 31)) * 16, ga0 = hh ? L_ZB : L_GA + (32 * w + lane) * 8, ga1 = hh ? L_GA + (32 * w + lane - 32) * 8 : L_ZB, csa = L_CS + lane * 16, csw = csa + w * 4;
    u32 tE, tO, tV, tU;
    {
      const u32 r = (lane >> 2) & 3, t0 = (32 * (w & 1) + 16 * hh + r) * 128 + (w >> 1) * 64 + (lane & 16) * 2 + (lane & 3) * 8;
      tE = L_K + 128 + (t0 ^ (((r + 1) & 2) << 5)); tO = L_K + 256 + (t0 ^ (((r + 2) & 2) << 5)); tV = L_V + (t0 ^ ((r & 2) << 5));
      tU = L_UT + (32 * (w >> 1) + (lane & 31)) * 128 + (((6 - 4 * (w & 1) - 2 * hh) ^ (lane & 7)) << 4);
    }
    uta = L_UT + (tid >> 3) * 256 + (((tid ^ (tid >> 2)) & 6) << 4) + (tid & 1) * 16;
    f32x2 tt[8];
#pragma unroll
    for (int a = 0; a < 8; ++a) { tt[a] = f32x2{0.f, 0.f}; asm volatile("" : "+v"(tt[a])); }
    asm volatile("" : "+v"(tE), "+v"(tO), "+v"(tV), "+v"(tU), "+v"(uta));
#pragma unroll
    for (int k = 0; k < 16; ++k) { acc0[k] = 0.f; acc1[k] = 0.f; }
    asm volatile("" : "+v"(acc0), "+v"(acc1), "+v"(cs0), "+v"(sel1), "+v"(exo), "+v"(gka), "+v"(ga0), "+v"(ga1), "+v"(csa), "+v"(csw));
    SB();
    STI(46);
#pragma unroll
    for (int x = 0; x < 2; ++x) dmai(w * 2048, L_XR + x * 1024, lane * 16, srdX, (128 * I + 8 * J + 2 * w) * 1024, x * 1024);
    asm volatile("buffer_load_dword %0, %1, %2, %3 offen" : "=&v"(pf1) : "v"(lane < 12 - (b & 1) && rr >= 4 ? lane * 128 : 0x80000000u), "s"(srdW1),
                 "s"((16 * rr + 2 * (b & 6) + w) * (F_ * 2) + (b & 1) * 1408) : "memory");
    asm volatile("buffer_load_ushort %0, %1, %2, %3 offen" : "=&v"(b1v) : "v"(lane * 2), "s"(mk_srd(kptr(ka1, 4), F_ * 2)), "s"(unit0 * 64) : "memory");
    asm volatile("" : "+s"(eR), "+s"(eC), "+s"(dvv), "+s"(t0) : "s"(b2t));
    const u32 ty = (u32)t0 + P_TY;
    tagR = next_tag(eR); tagC = next_tag(eC);
    asm volatile("" : "+s"(tagR), "+s"(tagC));
    ST(30);
    asm volatile("s_waitcnt vmcnt(8)" : "+{v[252:253]}"(sk) : "{v255}"(epf) : "memory");
    wg_barrier();
    ST(31);
    {
      u32 E[10], O[10], V[8];
#pragma unroll
      for (int n = 0; n < 5; ++n) {
        const u32x2 a = lds_tr(tE + n * 512), c = lds_tr(tO + n * 512);
        E[2 * n] = a.x; E[2 * n + 1] = a.y; O[2 * n] = c.x; O[2 * n + 1] = c.y;
      }
#pragma unroll
      for (int n = 0; n < 4; ++n) { const u32x2 v = lds_tr(tV + n * 512); V[2 * n] = v.x; V[2 * n + 1] = v.y; }
      const u32 s10 = __builtin_amdgcn_alignbit(sk.x, sk.x, 16), s32 = __builtin_amdgcn_alignbit(sk.y, sk.y, 16);
      SB();
#pragma unroll
      for (int a = 0; a < 8; ++a) { tt[a].y = dot2(E[a], s32, tt[a].y); tt[a].x = dot2(O[a], s32, tt[a].x); }
      SB();
#pragma unroll
      for (int a = 0; a < 8; ++a) { tt[a].y = dot2(E[a + 1], s10, tt[a].y); tt[a].x = dot2(O[a + 1], s10, tt[a].x); }
      SB();
      f32x2 vv[8];
#pragma unroll
      for (int a = 0; a < 8; ++a) vv[a] = f32x2{bhi(V[a]), blo(V[a])};
      SB();
#pragma unroll
      for (int a = 0; a < 8; ++a) tt[a] *= vv[a];
      SB();
#pragma unroll
      for (int x = 0; x < 2; ++x) {
        u32x4 p;
#pragma unroll
        for (int d = 0; d < 4; ++d) p[d] = pk(tt[7 - 4 * x - d].x, tt[7 - 4 * x - d].y);
        lds_st<u32x4>(tU ^ (16 * x), p);
      }
      SB();
    }
    ST(1);
    wait_vm<4>();
    const int e = w >> 1, hf = w & 1;
    u32x4 xb[5], aa; u32x2 wf[5][4], bg0, bg1; Gk gk; f32x4 c0; f32x2 gq;
    auto rd = [&](int c, int j) {
      const int ks = j / 5, t = j % 5;
      const int kr = ks == 3 ? 3 + (c & 1) : ks;
      if (t == 0) xb[kr] = lds_ld<u32x4>((c & 3) * XS + xa[ks]);
      else wf[kr][t - 1] = lds_tr((c & 3) * WSL + ((t - 1) >> 1) * 64 + ((t - 1) & 1) * 768 + wk[ks]);
    };
    wg_barrier();
    ST(2);
#pragma unroll
    for (int j = 0; j < P_J4; ++j) rd(0, j);
    if (P_AP) __builtin_amdgcn_s_setprio(P_AP);
    SB();
    static_for<32>([&](auto S) {
      constexpr int s = S.value, r = (s & 3) == 3 ? 3 + ((s >> 2) & 1) : s & 3, c = (s + 2) >> 2, ph_ = (s + 2) & 3;
      constexpr int jb[7] = {0, P_J1, P_J2, P_J3, P_J4, P_J5, 20};
      static_assert(P_J1 <= 10, "war");
      if constexpr (ph_ == 0) {
        STL(20 + c - 1);
        if constexpr (c == 1) wait_vm<2>();
        if constexpr (c == 2) asm volatile("s_waitcnt vmcnt(0)" : "+v"(b1v), "+v"(uo[0]), "+v"(uo[1]) : "v"(pf1) : "memory");
        wg_barrier();
        if constexpr (c == 8) {
          __builtin_amdgcn_s_setprio(1);
          c0 = lds_ld<f32x4>(csa);
          bg0 = lds_ld<u32x2>(ga0); bg1 = lds_ld<u32x2>(ga1); gq = lds_ld<f32x2>(gka);
        }
        SB();
      }
      acc0 = mfma32(cat(wf[r][0], wf[r][1]), xb[r], acc0);
      SB();
      if constexpr (r == 0) { cs0 = mfma16(sel, cat(wf[r][0], wf[r][1]), cs0); SB(); }
      if constexpr (c < 8 && ph_ < 3) {
#pragma unroll
        for (int j = jb[2 * ph_]; j < jb[2 * ph_ + 1]; ++j) rd(c, j);
      }
      if constexpr (s == 1) { uo[0] = lds_ld<u32x4>(uta); uo[1] = lds_ld<u32x4>((uta ^ 16) + 128); }
      if constexpr (s == 29) lds_st<float>(csw, cs0[0]);
      if constexpr (s == 31) aa = aug_a(c0, blo(b1v));
      SB();
      acc1 = mfma32(cat(wf[r][2], wf[r][3]), xb[r], acc1);
      SB();
      if constexpr (r == 0) { cs0 = mfma16(sel1, cat(wf[r][2], wf[r][3]), cs0); SB(); }
      if constexpr (c < 8 && ph_ < 3) {
#pragma unroll
        for (int j = jb[2 * ph_ + 1]; j < jb[2 * ph_ + 2]; ++j) rd(c, j);
      }
      if constexpr (s == 31) {
        acc0 = mfma32(aa, cat(bg0, bg0), acc0);
        SB();
        acc1 = mfma32(aa, cat(bg1, bg1), acc1);
        SB();
        gk = gelu_k(gq.x, gq.y);
      }
      SB();
    });
    ST(5);
    gelu_half(acc0, 0, gk, rws, hbase + lane * 16);
    gelu_half(acc0, 1, gk, rws, hbase + lane * 16);
    ST(33);
    gelu_half(acc1, 0, gk, rws, hbase + lane * 16 + 2048);
    gelu_half(acc1, 1, gk, rws, hbase + lane * 16 + 2048);
    ST(7);
    {
      const int kq = lane >> 4, eb_ = ((lane >> 2) & 3) + kq;
      const u32 cb = L_HC + (4 * e + (lane & 3)) * HCS;
      u32 a1_ = cb + (eb_ + 1) * 272, a0_ = eb_ >= 3 ? cb + (eb_ - 3) * 272 : L_ZB;
      u32 bb = L_UL + e * ULS + (15 - (lane & 15)) * 16 + kq * 256;
      f32x4 acc[2][2];
#pragma unroll
      for (int k = 0; k < 4; ++k) { acc[0][0][k] = 0.f; acc[0][1][k] = 0.f; acc[1][0][k] = 0.f; acc[1][1][k] = 0.f; }
      u32x4 uu[2];
      u32 dlo = e ? dvv >> 16 : dvv & 0xffffu, dhi = dlo << 16, yso = (lane ^ 56) * 16;
      asm volatile("" : "+v"(dlo), "+v"(dhi), "+v"(yso));
      ST(9);
      conv_run(hf == 0, a0_, a1_, bb, acc, uu, L_UL + e * ULS + (504 - 8 * lane) * 2, 4 - P_PUB / 4, lane == 0 ? WS_FH + ((2 * I + e) * 16 + J) * P_FS + hf * 8 : 0x80000000u, tagR, srdS);
      ST(11);
#pragma unroll
      for (int x = 0; x < 2; ++x) {
        const int t = x == 0 ? 3 - hf : hf;
        const u32x4 u = uu[x];
        u32x4 o;
        o.x = pk(dot2(u.w, dhi, acc[x][0][0]), dot2(u.w, dlo, acc[x][0][1]));
        o.y = pk(dot2(u.z, dhi, acc[x][0][2]), dot2(u.z, dlo, acc[x][0][3]));
        o.z = pk(dot2(u.y, dhi, acc[x][1][0]), dot2(u.y, dlo, acc[x][1][1]));
        o.w = pk(dot2(u.x, dhi, acc[x][1][2]), dot2(u.x, dlo, acc[x][1][3]));
        __builtin_amdgcn_raw_buffer_store_b128(o, rws, yso, WS_U + (ch0 + e) * UP + (3 - t) * 1024, P_YA);
        SB();
      }
      STI(44);
      lds_post<1>(L_CNT + 12, 1u);
      __builtin_amdgcn_s_setprio(0);
      const u32x4 ex = __builtin_amdgcn_raw_buffer_load_b128(mk_rs(kptr(ka0, 4), S_ * D_ * 2), exo, 0, 0);
      const u32x4 eb = __builtin_amdgcn_raw_buffer_load_b128(mk_rs(kptr(ka1, 7), D_ * 2), exo & 1023, 0, 0);
      SB();
      ST(12);
      if (P_EE) epoch();
      if (P_YI) {
#pragma unroll 1
        for (int it = 0; it < 64; ++it) {
          if ((u32)__builtin_amdgcn_readfirstlane(__hip_atomic_load((AS3 u32*)(uintptr_t)(L_CNT + 8), __ATOMIC_RELAXED, __HIP_MEMORY_SCOPE_WORKGROUP)) >= 4 + P_YN) break;
          __builtin_amdgcn_s_sleep(2);
        }
      } else if (P_TY) {
#pragma unroll 1
        for (int it = 0; it < P_SL0 && (int)((u32)__builtin_amdgcn_s_memtime() - ty) < 0; ++it) __builtin_amdgcn_s_sleep(1);
      } else if (P_SL0) __builtin_amdgcn_s_sleep(P_SL0);
      ST(35);
      yload();
      SB();
      {
        const f32x2 ms = pre_ms();
        pre_adr();
#pragma unroll
        for (int d = 0; d < 8; ++d) pre_epi(d, ex, eb, ms);
      }
      ya = L_UL + (tid & 7) * 1024 + (((tid >> 5) ^ (tid & 7)) << 4) + ((tid >> 3) & 3) * 4;
      asm volatile("" : "+v"(ya));
      lds_wait(L_CNT + 12, 4);
      SB();
      wait_vm<2>();
      STI(47);
      __builtin_amdgcn_raw_buffer_store_b64(split(tagC), rws, lane == 0 ? WS_FU + (tj * 32 + ti) * P_FS + 32 + w * 8 : 0x80000000u, 0, 16);
      pend = __any(same8(y0, uo[0]) | same8(y1, uo[1]));
      if (pend) {
        if (P_SLB) __builtin_amdgcn_s_sleep(P_SLB);
        asm volatile("" ::: "memory");
        yload();
      } else ytrans();
      cwa = L_CNT; cwn = 4;
    }
    ST(14);
  } else {
    indices();
    const char *phl = kptr(ka1, 2), *wsl = ws;
    asm volatile("" : "+s"(phl), "+s"(wsl)::"memory");
    u64 eR = *(const AS4 u64*)(wsl + WS_EROW + I * 128), eC = *(const AS4 u64*)(wsl + WS_ECOL + tj * 128);
    u32x2 hB[2];
#pragma unroll
    for (int x = 0; x < 2; ++x) hB[x] = *(const AS4 u32x2*)(phl + (ch0 + x) * 4096 + max(w * 1024 - 8, 0));
    SB();
    const int lane = tid & 63, hh = lane >> 5, mu = tid & 255;
    sx0 = (128 * I + 32 * w) * 1024; sw0 = 16 * w * (F_ * 2) + unit0 * 64;
    u32 l16 = lane * 16;
    u32x4 t4[2] = {{0, 0, 0, 0}, {0, 0, 0, 0}};
    const __amdgpu_buffer_rsrc_t rws = mk_rs(ws, WS_END);
    const u32x4 srdW2 = mk_srd(kptr(ka1, 6), D_ * F_ * 2);
    const bool full = w < 3;
    const int kb = 22 * w;
    u32 s20 = kb * 16384 + tj * 128;
    u32x4 gr = {0, 0, 0, 0}, ub[2]; u32x2 f0 = {0, 0};
    u32 gro, fuo, pfo, yvo, pf2 = 0, b1v = 0;
    auto issue = [&](auto G) {
      constexpr int g = G.value;
      if constexpr (g < 56) g1dma(G);
      else dmai(g % 7 < 4 ? dX : dW, pofs(g), vW2, srdW2, s20, (g - 56) * 8192);
      if (g == P_XH) {
#pragma unroll
        for (int x = 0; x < 2; ++x) ld16(t4[x], l16, mk_srd(kptr(ka1, 2), S_ * D_ * 2), (ch0 + x) * 4096 + w * 1024);
        asm volatile("buffer_load_ushort %0, %1, %2, %3 offen" : "+v"(b1v) : "v"((l16 >> 3) & 62), "s"(mk_srd(kptr(ka1, 4), F_ * 2)), "s"(unit0 * 64 + 128) : "memory");
      }
      if (g == P_XW) asm volatile("buffer_load_dword %0, %1, %2, 0 offen" : "+v"(pf2) : "v"(pfo), "s"(srdW2) : "memory");
      if (g == P_XG) ld16c(gr, gro, srdS);
      if (g == P_XF) ld8c(f0, fuo, srdS);
      SB();
    };
    mkvw(2);
    setup(lane, 1);
    f32x16 acc2;
#pragma unroll
    for (int k = 0; k < 16; ++k) acc2[k] = 0.f;
    f32x4 cs2 = {0.f, 0.f, 0.f, 0.f};
    exo = (64 * ti + (tid >> 3)) * 1024 + tj * 128 + (tid & 7) * 16;
    gro = WS_ST + (128 * I + 32 * w + (lane & 31)) * 16;
    fuo = WS_FU + (tj * 32 + 16 * (1 - (w & 1)) + (lane & 15)) * P_FS;
    pfo = (lane & 15) >= 11 || lane >= 16 + 16 * P_C3 ? 0x80000000u : lane < 16 ? (b * 4 + w) * 1376 + lane * 128
        : ((rr * 4 + w) * 11 + lane - 16) * 1024 + ((3 - (srdW2.x >> 7)) & 7) * 128;
    yvo = WS_U + (64 * tj + 2 * (mu >> 3)) * UP + (31 - ti) * 128 + (mu & 7) * 16;
    uta = L_UT + (mu >> 3) * 256 + (((mu ^ (mu >> 2)) & 6) << 4) + (mu & 1) * 16;
    u32 gso = (lane & 15) == 0 ? WS_ST + (128 * I + 8 * J + 2 * w) * 16 + (lane >> 4) * 8 : 0x80000000u;
    u32 fo0 = mu == 0 ? WS_FU + (tj * 32 + ti) * P_FS : 0x80000000u;
    asm volatile("" : "+v"(vw[2]), "+v"(acc2), "+v"(cs2), "+v"(l16), "+v"(t4[0]), "+v"(t4[1]), "+v"(exo), "+v"(gro), "+v"(fuo), "+v"(pfo));
    asm volatile("" : "+v"(uta), "+v"(yvo), "+v"(gso), "+v"(fo0));
    asm volatile("" : "+s"(sx0), "+s"(sw0), "+s"(s20));
    SB();
    ST(30);
    asm volatile("s_waitcnt vmcnt(4)\n\ts_barrier" ::"{v255}"(epf), "{v[252:253]}"(sk) : "memory");
    ST(31);
    static_for<P_N0, 7>(issue);
    asm volatile("" : "+s"(hB[0]), "+s"(hB[1]), "+s"(eR), "+s"(eC));
    tagR = next_tag(eR); tagC = next_tag(eC);
    u32 gtag = (lane & 16) ? (u32)(tagR >> 32) : (u32)tagR;
    asm volatile("" : "+s"(tagR), "+s"(tagC), "+v"(gtag));
    static_assert(P_HC == 30 || P_HC == 31, "filter copies");
    static_assert(P_N0 >= 7 && P_N0 <= 28 && P_S1 >= 2 && P_US >= 1, "n0");
    static_assert(P_XG < 7 * ((P_SG + 2) >> 2) + 6 && P_SG + 4 < 30 && P_XF < gend(imin(P_SU, 31)), "side jobs");
    static_assert(gend(P_US + 7 / P_UD) <= 7 * ((P_AG + 2) >> 2) + 6 && P_AG < 30, "u flag");
    __builtin_amdgcn_s_setprio(2);
    ST(1);
    wait_vm<nops(7, P_N0)>();
    u32x4 xb[5], baug, aa; u32x2 wf[5][2]; Gk gk; f32x4 c2;
    auto rd = [&](int c, int j) {
      const int ks = j / 3, t = j % 3;
      const int kr = ks == 3 ? 3 + (c & 1) : ks;
      if (t == 0) xb[kr] = lds_ld<u32x4>((c & 3) * XS + xa[ks]);
      else wf[kr][t - 1] = lds_tr((c & 3) * WSL + (t - 1) * 768 + wk[ks]);
    };
    auto hcopy = [&](int x) {
      const u32x4 P = t4[x];
      const u32 Q0 = __builtin_amdgcn_update_dpp(w ? hB[x].x : 0u, P.z, 0x138, 0xf, 0xf, false);
      const u32 Q1 = __builtin_amdgcn_update_dpp(w ? hB[x].y : 0u, P.w, 0x138, 0xf, 0xf, false);
      const u32 s0 = __builtin_amdgcn_alignbit(Q1, Q0, 16), s1 = __builtin_amdgcn_alignbit(P.x, Q1, 16),
                s2 = __builtin_amdgcn_alignbit(P.y, P.x, 16), s3 = __builtin_amdgcn_alignbit(P.z, P.y, 16),
                s4 = __builtin_amdgcn_alignbit(P.w, P.z, 16);
      const u32 ad = L_HC + 4 * x * HCS + 32 + (mu + (mu >> 4)) * 16;
      lds_st<u32x4>(ad, u32x4{s0, s1, s2, s3});
      lds_st<u32x4>(ad + HCS, u32x4{Q1, P.x, P.y, P.z});
      lds_st<u32x4>(ad + 2 * HCS, u32x4{s1, s2, s3, s4});
      lds_st<u32x4>(ad + 3 * HCS, P);
      if (x == 0 && mu < 8) lds_st<u32x4>(L_HC + mu * HCS, u32x4{0, 0, 0, 0});
    };
    wg_barrier();
    ST(2); STX(56);
#pragma unroll
    for (int j = 0; j < 2 * P_JB; ++j) rd(0, j);
    SB();
    bool ulate = false;
    auto udma = [&]() {
      const u32 ul = L_UL + (w >> 1) * ULS + (w & 1) * 2048, uo = WS_U + (ch0 + (w >> 1)) * UP + (w & 1) * 2048;
      dma16c(ul, l16, srdS, uo);
      dma16c(ul + 1024, l16, srdS, uo + 1024);
    };
    auto upoll = [&]() {
#pragma unroll 1
      for (int it = 0; it < P_SPIN; ++it) {
        if (__all(f0.x == (u32)tagC && f0.y == (u32)(tagC >> 32))) break;
        ld8c(f0, fuo, srdS);
        asm volatile("s_waitcnt vmcnt(0)" : "+v"(f0)::"memory");
      }
    };
    auto ufetch = [&](auto N) {
      asm volatile("s_waitcnt vmcnt(%1)" : "+v"(f0) : "i"(imin(63, N.value)) : "memory");
      if (P_DF) {
        ulate = !__all(f0.x == (u32)tagC && f0.y == (u32)(tagC >> 32));
        if (__builtin_expect(ulate, 0)) {
          ld8c(f0, fuo, srdS);
          asm volatile("buffer_store_dword %0, %0, %1, 0 offen" ::"v"(0x80000000u), "s"(srdS) : "memory");
        } else udma();
      } else { upoll(); udma(); }
      SB();
    };
    u32x4 xr[2]; float rs[2], rq[2], sm, r1, rr_;
    static_for<32>([&](auto S) {
      constexpr int s = S.value, r = (s & 3) == 3 ? 3 + ((s >> 2) & 1) : s & 3, c = (s + 2) >> 2, ph_ = (s + 2) & 3;
      if constexpr (ph_ == 0) {
        STL(20 + c - 1);
        if constexpr (c < 8) wait_vm<younger(7 * c + 6, ncum(c)) + nu(s, 7 * c + 6)>();
        STL(47 + c);
        wg_barrier();
        if constexpr (c == 8) c2 = lds_ld<f32x4>(L_CS + 1024 + (lane & 31) * 16);
        SB();
      }
      constexpr int ga = s == 0 ? P_N0 : gend(s - 1), gb = gend(s);
      static_assert(ncum(c + 1) <= 7 * (c + 4) && ncum(c + 1) >= ncum(c) && ncum(c) >= imin(7 * (c + 1), 56), "ring");
      acc2 = mfma32(cat(wf[r][0], wf[r][1]), xb[r], acc2);
      SB();
      if constexpr (r == 0) { cs2 = mfma16(sel, cat(wf[r][0], wf[r][1]), cs2); SB(); }
      if constexpr (c < 8 && ph_ < 3) {
#pragma unroll
        for (int j = P_JB * ph_; j < imin(12, P_JB * ph_ + P_JB); ++j) rd(c, j);
      }
      if constexpr (s == 29) { if (lane < 32) lds_st<float>(L_CS + 1024 + w * 4 + lane * 16, cs2[0]); }
      if constexpr (s == 30) {
        asm volatile("s_waitcnt vmcnt(%1)" : "+v"(b1v) : "i"(imin(63, younger(P_XH, ncum(8)) - 3 + nu(s, P_XH))) : "memory");
        aa = aug_a(c2, blo(b1v));
      }
      if constexpr (s == 31) acc2 = mfma32(aa, baug, acc2);
      SB();
      if constexpr (s == 0) { ub[0] = lds_ld<u32x4>(uta); ub[1] = lds_ld<u32x4>((uta ^ 16) + 128); }
      if constexpr (s == P_S1) {
#pragma unroll
        for (int x = 0; x < 2; ++x) xr[x] = lds_ld<u32x4>(L_XR + w * 2048 + x * 1024 + l16);
        SB();
      }
      static_for<gb, ga>(issue);
      if constexpr (s >= P_US && s < P_US + 8 / P_UD) {
        constexpr int k = (s - P_US) * P_UD;
        if (P_UD == 8) {
          __builtin_amdgcn_raw_buffer_store_b128(ub[0], rws, yvo, 0, 16);
          __builtin_amdgcn_raw_buffer_store_b128(ub[1], rws, yvo, UP, 16);
        } else if ((u32)(w - (k & 3)) < P_UD) __builtin_amdgcn_raw_buffer_store_b128(ub[k >> 2], rws, yvo, (k >> 2) * UP, P_UA);
      }
      if constexpr (s == P_S1 + 1) {
#pragma unroll
        for (int x = 0; x < 2; ++x) {
          rs[x] = 0.f; rq[x] = 0.f;
#pragma unroll
          for (int d = 0; d < 4; ++d) { const u32 t = xr[x][d]; rs[x] = dot2(t, ONES, rs[x]); rq[x] = dot2(t, t, rq[x]); }
        }
        float gran = swap16sum(swap32sum(rs[0], rs[1]), swap32sum(rq[0], rq[1]));
        gran = dppadd<0x128>(gran); gran = dppadd<0x124>(gran); gran = dppadd<0x122>(gran); gran = dppadd<0x121>(gran);
        u32x2 g; g.x = f2u(gran); g.y = gtag;
        __builtin_amdgcn_raw_buffer_store_b64(g, rws, gso, 0, 16);
      }
      if constexpr (s == P_AG) __builtin_amdgcn_raw_buffer_store_b64(split(tagC), rws, fo0, 0, 16);
      SB();
      if constexpr (s == 31) ST(32);
      if constexpr (s == P_HC || s == P_HC + 1) {
        asm volatile("" : "+v"(t4[0]), "+v"(t4[1]));
        hcopy(s - P_HC);
        SB();
      }
      if constexpr (s == P_SU) ufetch(IC<nops(P_XF + 1, gb)>{});
      if constexpr (s == P_SG) {
        asm volatile("" : "+v"(gr));
#pragma unroll 1
        for (int it = 0; it < P_SPIN; ++it) {
          if (tagged(gr, tagR)) break;
          ld16c(gr, gro, srdS);
          asm volatile("s_waitcnt vmcnt(0)" : "+v"(gr)::"memory");
        }
        sm = u2f(gr.x) * (1.f / 512.f);
        rr_ = __builtin_fmaxf(__builtin_fmaf(u2f(gr.z), 1.f / 512.f, -sm * sm), 0.f);
        asm volatile("" : "+v"(sm), "+v"(rr_));
      }
      if constexpr (s == P_SG + 1) { r1 = __builtin_amdgcn_rsqf(rr_ + 1e-5f); asm volatile("" : "+v"(r1)); }
      if constexpr (s == P_SG + 2) { rr_ = r1 * __builtin_amdgcn_rsqf(__builtin_fmaf(rr_ * r1, r1, 1e-5f)); asm volatile("" : "+v"(rr_)); }
      if constexpr (s == P_SG + 3) { gk = gelu_k(rr_, __builtin_amdgcn_rcpf(rr_)); asm volatile("" : "+v"(gk.k1), "+v"(gk.k2), "+v"(gk.ir)); }
      if constexpr (s == P_SG + 4) {
        const u32 h = pk(-sm, gk.ir), l = pk(-sm - blo(h), gk.ir - bhi(h));
        if (lane < 32) {
          lds_st<f32x4>(L_MS + w * 512 + l16, f32x4{sm, r1, rr_, gk.ir});
          lds_st<u32x2>(L_GA + w * 256 + lane * 8, u32x2{h, l});
        }
        baug.x = hh ? 0u : h; baug.y = hh ? 0u : l; baug.z = baug.x; baug.w = 0u;
        asm volatile("" : "+v"(baug));
      }
      SB();
    });
    static_assert(P_LATE <= 21 && P_G0 >= P_LATE && P_G0 <= 28 && P_XH < ncum(8), "late");
    ST(5); STX(57);
    static_assert(P_SU == 31 && P_HC == 31, "u");
    constexpr int a1 = P_G0 + P_I0, a2 = a1 + P_I1, a3 = a2 + P_PRE, a4 = a3 + P_I2;
    auto nost = [&]() { asm volatile("buffer_store_dword %0, %0, %1, 0 offen" ::"v"(0x80000000u), "s"(srdS) : "memory"); };
    static_for<56 + a1, 56 + P_G0>(issue);
    if (__builtin_expect(light, 0)) nost(); else gelu_half(acc2, 0, gk, rws, hbase + 4096 + l16);
    static_for<56 + a2, 56 + a1>(issue);
    if (__builtin_expect(light, 0)) nost(); else gelu_half(acc2, 1, gk, rws, hbase + 4096 + l16);
    ST(7);
    if (P_DF && __builtin_expect(ulate, 0)) {
      asm volatile("s_waitcnt vmcnt(%1)" : "+v"(f0) : "i"(3 + a2 - P_G0) : "memory");
      upoll();
      udma();
    }
    static_for<56 + a3, 56 + a2>(issue);
    hcopy(1);
    SB();
    static_for<56 + a4, 56 + a3>(issue);
    u32 w2a[2][2];
    {
      const int row = 4 * hh + ((lane >> 2) & 3);
#pragma unroll
      for (int dt = 0; dt < 2; ++dt) {
        const u32 a = row * 128 + ((dt ^ ((row >> 1) & 1)) << 6) + ((lane >> 4) & 1) * 32 + (lane & 3) * 8;
        w2a[dt][0] = a + dX; w2a[dt][1] = a + dW;
      }
    }
    auto w2half = [&](int p, int dt) { return lds_tr(w2a[dt][(p % 28) % 7 >= 4] + pofs(p % 28)); };
    auto w2frag = [&](int u, int dt) { return cat(w2half(2 * u, dt), w2half(2 * u + 1, dt)); };
    u32x4 bn[8][2];
    asm volatile("" : "+v"(w2a[0][0]), "+v"(w2a[0][1]), "+v"(w2a[1][0]), "+v"(w2a[1][1]));
    wait_vm<4 + a4 - P_G0>();
#pragma unroll
    for (int u = 0; 2 * u + 2 <= P_G0; ++u) { bn[u][0] = w2frag(u, 0); bn[u][1] = w2frag(u, 1); }
    SB();
    wait_vm<2 + a4 - P_G0>();
    if (P_DF && __builtin_expect(ulate, 0)) wait_vm<a4 - a2>();
    lds_post<1>(L_CNT + 4, 1u);
    if constexpr (P_EC > P_G0) {
      static_assert(P_EC <= a1, "ec");
      wait_vm<2 + a4 - P_EC>();
#pragma unroll
      for (int u = P_G0 / 2; 2 * u + 2 <= P_EC; ++u) { bn[u][0] = w2frag(u, 0); bn[u][1] = w2frag(u, 1); }
      SB();
    }
    __builtin_amdgcn_s_setprio(P_WP);
    ST(8);
    static_for<56 + P_G1, 56 + a4>(issue);
    asm volatile("s_waitcnt vmcnt(%1)" : "+v"(pf2) : "i"(P_G1 - a2) : "memory");
    lds_post<1>(L_CNT + 8, 1u);
    ST(34); STX(58);
    const u32 fpo = WS_FH + (ti * 16 + 4 * w + ((lane >> 1) & 3)) * P_FS + (lane & 1) * 8;
    u32x4 q[4];
    const char* fps = ws + WS_FH + (ti * 16 + 4 * w) * P_FS;
    auto spoll = [&]() {
      asm volatile("s_load_dwordx4 %0, %4, %5 glc\n\ts_load_dwordx4 %1, %4, %6 glc\n\ts_load_dwordx4 %2, %4, %7 glc\n\ts_load_dwordx4 %3, %4, %8 glc"
                   : "=&s"(q[0]), "=&s"(q[1]), "=&s"(q[2]), "=&s"(q[3])
                   : "s"(fps), "i"(0), "i"(P_FS), "i"(2 * P_FS), "i"(3 * P_FS) : "memory");
    };
    auto sok = [&]() {
      asm volatile("s_waitcnt lgkmcnt(0)" : "+s"(q[0]), "+s"(q[1]), "+s"(q[2]), "+s"(q[3])::"memory");
      u64 da = tagR, dr = tagR;
#pragma unroll
      for (int j = 0; j < 8; ++j) {
        const u64 t = ((u64)q[j >> 1][2 * (j & 1) + 1] << 32) | q[j >> 1][2 * (j & 1)];
        asm("s_and_b64 %0, %0, %2\n\ts_or_b64 %1, %1, %2" : "+s"(da), "+s"(dr) : "s"(t) : "scc");
      }
      return da == dr;
    };
    static_for<44, P_G1>([&](auto Pc) {
      constexpr int p = Pc.value;
      static_for<8>([&](auto U) {
        constexpr int u = U.value, pr = 2 * u + 2 + P_BL < P_G1 ? P_G1 : imin(2 * u + 2 + P_BL, 28);
        if constexpr (pr == p && 2 * u + 2 > P_EC) {
          wait_vm<p - 2 * u - 2>();
          if (P_BP != P_WP) { __builtin_amdgcn_s_setprio(P_BP); SB(); }
          bn[u][0] = w2frag(u, 0); bn[u][1] = w2frag(u, 1);
          SB();
          if (P_BP != P_WP) { __builtin_amdgcn_s_setprio(P_WP); SB(); }
        }
      });
      if constexpr (p >= 28 && (p & 1) == 0) asm volatile("" : "+v"(bn[(p - 28) / 2][0]), "+v"(bn[(p - 28) / 2][1]));
      if constexpr (p == P_WQ) __builtin_amdgcn_s_setprio(2);
      if (p < 40 || full) issue(IC<56 + p>{});
      if constexpr (p == P_FP && !P_SP) ld8c(f0, fpo, srdS);
      if constexpr (p == P_FP && P_SP) {
        asm volatile("" : "+v"(bn[7][0]), "+v"(bn[7][1]));
        spoll();
      }
      SB();
    });
    ST(9);
    bool ok = false;
    if (P_SP) {
      ok = sok();
      ST(36);
#pragma unroll 1
      for (int it = 0; it < P_SP && !ok; ++it) { spoll(); ok = sok(); }
      f0.x = ~(u32)tagR;
      ST(37);
    } else {
      asm volatile("s_waitcnt vmcnt(%1)" : "+v"(f0) : "i"(39 - P_FP) : "memory");
      ST(36);
    }
    if (!ok) {
#pragma unroll 1
      for (int it = 0; it < P_SPIN; ++it) {
        if (__all(f0.x == (u32)tagR && f0.y == (u32)(tagR >> 32))) break;
        ld8c(f0, fpo, srdS);
        asm volatile("s_waitcnt vmcnt(0)" : "+v"(f0)::"memory");
      }
    }
    ST(35);
    const u32 ho = WS_H + 2 * ti * 86 * 1024;
    u32x4 hb[P_WR], ex, eb; f32x2 ms;
    auto hload = [&](auto Ic) {
      constexpr int i = Ic.value, u = gu(i), x = gx(i);
      if constexpr (i < 44) {
        const u32 vo = (u < 20 || full) ? l16 : 0x80000000u;
        hb[i % P_WR] = __builtin_amdgcn_raw_buffer_load_b128(rws, vo, ho + (kb + u + x * 86) * 1024, P_HA);
      }
      if (i == P_EI + P_WA) ex = __builtin_amdgcn_raw_buffer_load_b128(mk_rs(kptr(ka0, 4), S_ * D_ * 2), exo, 0, 0);
      if (i == P_EI + P_WA + 1) eb = __builtin_amdgcn_raw_buffer_load_b128(mk_rs(kptr(ka1, 7), D_ * 2), exo & 1023, 0, 0);
    };
    static_for<gl(0)>(hload);
    if (P_WP || P_WQ < 44) __builtin_amdgcn_s_setprio(0);
    SB();
    ST(10); STX(59);
    f32x16 o[2][2];
#pragma unroll
    for (int k = 0; k < 16; ++k) { o[0][0][k] = 0.f; o[0][1][k] = 0.f; o[1][0][k] = 0.f; o[1][1][k] = 0.f; }
    u32 pw;
    {
      const int t = lane & 31, f = ((t >> 1) & 3) | ((t & 1) << 2);
      pw = dX + t * 128 + ((hh ^ f) << 4);
    }
    auto part = [&](int dt, int tt, int g) {
      f32x4 v; v.x = o[dt][tt][4 * g]; v.y = o[dt][tt][4 * g + 1]; v.z = o[dt][tt][4 * g + 2]; v.w = o[dt][tt][4 * g + 3];
      lds_st<f32x4>((2 * dt + tt) * XS + (pw ^ (g << 5)), v);
    };
    static_assert(P_TS >= 4 && P_TS <= 8 && P_WA < P_WR && P_EI + P_WA + 1 < gl(imin(P_PB, 43)) && P_PB >= 36 && P_PB <= 44 && (P_PS == 2 || P_TS == 8), "g2");
    static_for<44>([&](auto Ic) {
      constexpr int i = Ic.value, u = gu(i), x = gx(i), j = i - (44 - P_TS);
      const u32x4 a0 = bn[u & 7][0], a1 = bn[u & 7][1], h = hb[i % P_WR];
      o[0][x] = mfma32(a0, h, o[0][x]);
      SB();
      hload(IC<gl(i)>{});
      if (x == 1 && u + 8 < 22) bn[u & 7][0] = w2frag(u + 8, 0);
      if constexpr (P_PS == 2 && j >= 0 && j < 4) part(j >> 1, 0, 2 * (j & 1));
      if constexpr (P_PS == 1 && j >= 0) part(j >> 2, 0, j & 3);
      SB();
      o[1][x] = mfma32(a1, h, o[1][x]);
      SB();
      if constexpr (gl(i + 1) > gl(i) + 1) hload(IC<gl(i) + 1>{});
      if (x == 1 && u + 8 < 22) bn[u & 7][1] = w2frag(u + 8, 1);
      if constexpr (P_PS == 2 && j >= 0 && j < 4) part(j >> 1, 0, 2 * (j & 1) + 1);
      if (j == 8 / P_PS - 1) lds_post<1>(L_CNT, 1u);
      if (P_YI && i == P_YI) lds_post<1>(L_CNT + 8, 1u);
      if (i == 31 && !full) { bn[4][0] = u32x4{0, 0, 0, 0}; bn[4][1] = bn[4][0]; bn[5][0] = bn[4][0]; bn[5][1] = bn[4][0]; }
      if (i == P_TPI) __builtin_amdgcn_s_setprio(2);
      if (i == P_PA) pre_adr();
      if (i == imin(P_PA, P_PB - 1)) ms = pre_ms();
      if (i >= P_PB) pre_epi(i - P_PB, ex, eb, ms);
      SB();
    });
    asm volatile("" ::"v"(pf2));
    ST(11); STX(60);
#pragma unroll
    for (int k = 0; k < 8; ++k) {
      part(k >> 2, 1, k & 3);
      SB();
      if (k + 44 - P_PB < 8) pre_epi(k + 44 - P_PB, ex, eb, ms);
      SB();
    }
    lds_post<1>(L_CNT + 4, 1u);
    cwa = L_CNT + 4; cwn = 8;
    asm volatile("" : "=v"(y0), "=v"(y1), "=v"(uo[0]), "=v"(uo[1]), "=v"(ya));
    __builtin_amdgcn_s_waitcnt(0x0f70);
    asm volatile("Lcode_end:");
    ST(14);
  }
  if (P_SPN) lds_spin<P_SPN == 2 ? 4 : 0>(cwa, cwn); else lds_wait(cwa, cwn);
  ST(15);
  {
    f32x4 pa0[4], pa1[4];
#pragma unroll
    for (int ww = 0; ww < 4; ++ww) { pa0[ww] = lds_ld<f32x4>(ep0 + ww * 4096); pa1[ww] = lds_ld<f32x4>(ep1 + ww * 4096); }
    SB();
    f32x4 oa = {b2f[0], b2f[1], b2f[2], b2f[3]}, ob = {b2f[4], b2f[5], b2f[6], b2f[7]};
#pragma unroll
    for (int ww = 0; ww < 4; ++ww) { oa += pa0[ww]; ob += pa1[ww]; }
    f32x2 g[4];
    asm volatile("" : "+v"(oa), "+v"(ob) :: "memory");
    const u32 yc = __hip_atomic_load((AS3 u32*)(uintptr_t)(L_CNT + 12), __ATOMIC_RELAXED, __HIP_MEMORY_SCOPE_WORKGROUP);
    asm volatile("" ::: "memory");
    u32x4 y = lds_ld<u32x4>(epy);
    SB();
#pragma unroll
    for (int d = 0; d < 4; ++d) {
      f32x2 o; o.x = d < 2 ? oa[2 * d] : ob[2 * d - 4]; o.y = d < 2 ? oa[2 * d + 1] : ob[2 * d - 3];
      const f32x2 z = o * -1.4426950409f;
      f32x2 e; e.x = __builtin_amdgcn_exp2f(z.x); e.y = __builtin_amdgcn_exp2f(z.y);
      e += 1.f;
      f32x2 q; q.x = __builtin_amdgcn_rcpf(e.x); q.y = __builtin_amdgcn_rcpf(e.y);
      g[d] = f32x2{xm[2 * d], xm[2 * d + 1]} * o * q;
      asm volatile("" : "+v"(g[d]));
    }
    ST(18);
    if (__builtin_expect((u32)__builtin_amdgcn_readfirstlane(yc) < 8, P_XP)) {
    if (pend) {
      const char* fp = ws + WS_FU + (tj * 32 + (tid >> 3)) * P_FS + 32 + (tid & 1) * 16 + (((ti >> 3) ^ (ti >> 4)) & 1) * 8;
      bool last = false;
#pragma unroll 1
      for (int it = 1; it < P_SPIN; ++it) {
        if (last || !__any(same8(y0, uo[0]) | same8(y1, uo[1]))) break;
        __builtin_amdgcn_s_sleep(P_SLA);
        if (it >= P_NTRY) last = __all(__hip_atomic_load((const AS1 u64*)fp, RLX_AGENT) == tagC);
        asm volatile("" ::: "memory");
        yload();
      }
      ytrans();
    }
    if (P_SPY) lds_spin<0>(L_CNT + 12, 8); else lds_wait(L_CNT + 12, 8);
    ST(19);
    y = lds_ld<u32x4>(epy);
    }
    u32x4 ov;
#pragma unroll
    for (int d = 0; d < 4; ++d) {
      const f32x2 r = __builtin_elementwise_fma(g[d], f32x2{blo(y[d]), bhi(y[d])}, f32x2{xf[2 * d], xf[2 * d + 1]});
      ov[d] = pk(r.x, r.y);
    }
    __builtin_amdgcn_raw_buffer_store_b128(ov, mk_rs(pout, S_ * D_ * 2), exo, 0, P_OAUX);
    SB();
    asm volatile("" ::"v"(y0), "v"(y1));
    if (!P_EE) epoch();
  }
}

extern "C" void kernel_launch(void* const* d_in, const int* in_sizes, int n_in, void* d_out, int out_size, void* d_ws,
                              size_t ws_size, hipStream_t stream) {
  (void)hipFuncSetAttribute((const void*)fused_kernel, hipFuncAttributeMaxDynamicSharedMemorySize, L_END);
  KArgs a;
  a.k = Srd{(const char*)d_in[1], S_ * D_ * 2, 0x00020000u}; a.v = Srd{(const char*)d_in[2], S_ * D_ * 2, 0x00020000u};
  a.q = Srd{(const char*)d_in[0], S_ * D_ * 2, 0x00020000u}; a.w1 = Srd{(const char*)d_in[7], D_ * F_ * 2, 0x00020000u};
  a.ws = Srd{(const char*)d_ws, WS_END, 0x00020000u};
  a.h = (const char*)d_in[4]; a.sk = (const char*)d_in[3]; a.b1 = (const char*)d_in[8];
  a.D = (const char*)d_in[5]; a.w2 = (const char*)d_in[9]; a.b2 = (const char*)d_in[10]; a.out = (const char*)d_out;
  fused_kernel<<<256, 512, L_END, stream>>>(a);
}
```
